```python
import torch

def _softmax(x, axis=-1):
    return torch.softmax(x, dim=axis)
def _gelu(x):
    return torch.nn.functional.gelu(x, approximate="tanh")
def _silu(x):
    return torch.nn.functional.silu(x)
def _sigmoid(x):
    return torch.sigmoid(x)
def _layernorm(x, axis=-1, eps=1e-5):
    m = x.mean(dim=axis, keepdim=True)
    v = x.var(dim=axis, keepdim=True, unbiased=False)
    return (x - m) / torch.sqrt(v + eps)
def _scatter_add(tgt, idx, src, axis=0):
    return tgt.clone().index_add_(axis, idx, src)
def _segment_sum(src, seg, n):
    out = torch.zeros((n,) + src.shape[1:], dtype=src.dtype)
    return out.index_add_(0, seg, src)
def _pad(x, pw):
    flat = tuple(v for pair in reversed(pw) for v in pair)
    return torch.nn.functional.pad(x, flat)

kernel_name = 'concat_heads__mha_block_cached__mha_block_cached__near_ridge'

def setup_inputs():

    _w = (lambda a: (lambda t: t.to(torch.bfloat16) if t.is_floating_point() else t)(torch.as_tensor(a)))
    def _A_setup():
        B, Sq, D = 8, 208, 512
        H, Sk, Dh = 32, 512, 128
        return {
            "x": _w((lambda *s: torch.randn(*s, dtype=torch.bfloat16) * 0.8)(B, Sq, D)),
            "k_cache": _w((lambda *s: torch.randn(*s, dtype=torch.bfloat16) * 0.8)(B, H, Sk, Dh)),
            "v_cache": _w((lambda *s: torch.randn(*s, dtype=torch.bfloat16) * 0.05)(B, H, Sk, Dh)),
            "wq": _w((lambda *s: torch.randn(*s, dtype=torch.bfloat16) * 0.8)(D, H * Dh) * (D ** -0.5)),
            "wo": _w((lambda *s: torch.randn(*s, dtype=torch.bfloat16) * 0.05)(H * Dh, D) * ((H * Dh) ** -0.5)),
        }
    _A_in = _A_setup()

    def _B_setup():
        B, Sq, D = 8, 208, 512
        H, Sk, Dh = 32, 512, 128
        return {
            "x": _w((lambda *s: torch.randn(*s, dtype=torch.bfloat16) * 0.8)(B, Sq, D)),
            "k_cache": _w((lambda *s: torch.randn(*s, dtype=torch.bfloat16) * 0.8)(B, H, Sk, Dh)),
            "v_cache": _w((lambda *s: torch.randn(*s, dtype=torch.bfloat16) * 0.05)(B, H, Sk, Dh)),
            "wq": _w((lambda *s: torch.randn(*s, dtype=torch.bfloat16) * 0.8)(D, H * Dh) * (D ** -0.5)),
            "wo": _w((lambda *s: torch.randn(*s, dtype=torch.bfloat16) * 0.05)(H * Dh, D) * ((H * Dh) ** -0.5)),
        }
    _B_in = _B_setup()

    return {
        "A_x": _A_in["x"],
        "A_k_cache": _A_in["k_cache"],
        "A_v_cache": _A_in["v_cache"],
        "A_wq": _A_in["wq"],
        "A_wo": _A_in["wo"],
        "B_x": _B_in["x"],
        "B_k_cache": _B_in["k_cache"],
        "B_v_cache": _B_in["v_cache"],
        "B_wq": _B_in["wq"],
        "B_wo": _B_in["wo"],
    }

def reference(A_x, A_k_cache, A_v_cache, A_wq, A_wo, B_x, B_k_cache, B_v_cache, B_wq, B_wo):
    def _A_ref(x, k_cache, v_cache, wq, wo):
        B, Sq, D = x.shape
        H, Dh = 32, 128
        q = (lambda x, *d: x.permute(*d))((x @ wq).reshape(B, Sq, H, Dh), 0, 2, 1, 3)
        scores = torch.einsum("bhqd,bhkd->bhqk", q, k_cache) * (Dh ** -0.5)
        p = _softmax(scores, axis=-1)
        o = torch.einsum("bhqk,bhkd->bhqd", p, v_cache)
        return (lambda x, *d: x.permute(*d))(o, 0, 2, 1, 3).reshape(B, Sq, H * Dh) @ wo

    def _B_ref(x, k_cache, v_cache, wq, wo):
        B, Sq, D = x.shape
        H, Dh = 32, 128
        q = (lambda x, *d: x.permute(*d))((x @ wq).reshape(B, Sq, H, Dh), 0, 2, 1, 3)
        scores = torch.einsum("bhqd,bhkd->bhqk", q, k_cache) * (Dh ** -0.5)
        p = _softmax(scores, axis=-1)
        o = torch.einsum("bhqk,bhkd->bhqd", p, v_cache)
        return (lambda x, *d: x.permute(*d))(o, 0, 2, 1, 3).reshape(B, Sq, H * Dh) @ wo

    a = _A_ref(A_x, A_k_cache, A_v_cache, A_wq, A_wo)
    b = _B_ref(B_x, B_k_cache, B_v_cache, B_wq, B_wo)
    return torch.concatenate([a, b], axis=-1)
```

```cpp
#include <hip/hip_runtime.h>
#include <hip/hip_bf16.h>

typedef __attribute__((ext_vector_type(8))) short bf16x8;
typedef __attribute__((ext_vector_type(4))) short s16x4;
typedef __attribute__((ext_vector_type(4))) float f32x4;
typedef __attribute__((ext_vector_type(2))) float f32x2;
typedef __attribute__((ext_vector_type(4))) unsigned u32x4;
typedef __attribute__((ext_vector_type(2))) unsigned u32x2;
typedef __attribute__((ext_vector_type(2))) __bf16 bf16x2_t;
typedef short v4i16_t __attribute__((ext_vector_type(4)));
#define LDS_AS __attribute__((address_space(3)))

constexpr int NB_BATCH = 8, SQ = 208, DM = 512, NH = 32, SK = 512, DH = 128;
constexpr int NRB = 13;
constexpr float QSCALE = 0.08838834764831845f * 1.4426950408889634f;
constexpr float THR = 8.0f;

constexpr int KV_STAGE = 32768;
constexpr int X_SLOT = 13 * 1024, W_SLOT = 16384;
constexpr int X_RING = 65536, W_RING = X_RING + 3 * X_SLOT;
constexpr int LDS1 = W_RING + 3 * W_SLOT;

constexpr size_t WS_OF = 0;
constexpr size_t OF_BYTES = (size_t)16 * NRB * 128 * 1024;
constexpr size_t WS_WF = OF_BYTES;
constexpr size_t WF_BYTES = (size_t)2 * 128 * 32 * 1024;

#define MFMA16(a, b, c) __builtin_amdgcn_mfma_f32_16x16x32_bf16(a, b, c, 0, 0, 0)
#define WAIT_BAR(N) asm volatile("s_waitcnt vmcnt(" #N ") lgkmcnt(0)\n\ts_barrier" ::: "memory")

__device__ __forceinline__ void glds16(const char* sbase, unsigned voff, unsigned lds_base) {
    unsigned sv;
    asm volatile("s_mov_b32 %0, m0\n\ts_mov_b32 m0, %3\n\ts_nop 0\n\tglobal_load_lds_dwordx4 %1, %2\n\ts_mov_b32 m0, %0"
                 : "=&s"(sv) : "v"(voff), "s"(sbase), "s"(lds_base) : "memory");
}
__device__ __forceinline__ bf16x8 lds_b128(unsigned a) { return *(const LDS_AS bf16x8*)(a); }
__device__ __forceinline__ s16x4 lds_tr(unsigned a) {
    return __builtin_bit_cast(s16x4, __builtin_amdgcn_ds_read_tr16_b64_v4i16((LDS_AS v4i16_t*)(a)));
}
__device__ __forceinline__ bf16x8 cat44(s16x4 l, s16x4 h) { return (bf16x8){l[0], l[1], l[2], l[3], h[0], h[1], h[2], h[3]}; }
__device__ __forceinline__ unsigned cvtpk(float lo, float hi) {
    f32x2 v = {lo, hi};
    return __builtin_bit_cast(unsigned, __builtin_convertvector(v, bf16x2_t));
}
__device__ __forceinline__ bf16x8 pack8(f32x4 a, f32x4 b) {
    u32x4 w = {cvtpk(a[0], a[1]), cvtpk(a[2], a[3]), cvtpk(b[0], b[1]), cvtpk(b[2], b[3])};
    return __builtin_bit_cast(bf16x8, w);
}
__device__ __forceinline__ float max3f(float a, float b, float c) { return __builtin_fmaxf(__builtin_fmaxf(a, b), c); }

struct Ptrs {
    const __hip_bfloat16* x[2];
    const __hip_bfloat16* k[2];
    const __hip_bfloat16* v[2];
    const __hip_bfloat16* wq[2];
    const __hip_bfloat16* wo[2];
};

__device__ __forceinline__ int opaque_lane() {
    int l;
    asm volatile("v_mbcnt_lo_u32_b32 %0, -1, 0\n\tv_mbcnt_hi_u32_b32 %0, -1, %0" : "=v"(l));
    return l;
}
template <int N> __device__ __forceinline__ void wait_bar() {
    asm volatile("s_waitcnt vmcnt(%0) lgkmcnt(0)\n\ts_barrier" :: "n"(N) : "memory");
}
__device__ __forceinline__ int tr_colB_of(int lane) {
    const int tr_row = (lane & 31) >> 2, cpos = lane & 3;
    return (lane >> 5) * 64 + (((cpos >> 1) ^ (tr_row >> 2)) * 32) + (cpos & 1) * 16;
}

template <int NB>
__device__ __forceinline__ void attn_wave(const char* __restrict__ xg, const char* __restrict__ wqg, const char* __restrict__ kg,
                                          const char* __restrict__ vg, const char* __restrict__ wog, char* __restrict__ ofp,
                                          char* __restrict__ wfp, int w, int grp, int wi, int rb0) {
    const int lane = threadIdx.x & 63;
    const int m = lane & 15, g = lane >> 4, q4 = m >> 2, p4 = lane & 3, hh = lane >> 5, g1 = g & 1;
    const int tr_row = (lane & 31) >> 2;
    const int kkey = 8 * wi + (lane >> 3);
    unsigned k_lane = (unsigned)(kkey * 256 + (((lane & 7) ^ ((kkey >> 1) & 7)) * 16));
    unsigned v_lane = (unsigned)((8 * wi + tr_row) * 256 + tr_colB_of(lane));
    auto kv_one = [&](int st, int i) {
        const unsigned d = (unsigned)((st & 3) * KV_STAGE + grp * 16384);
        if (i < 2) glds16(kg + (size_t)st * 8192 + i * 128, k_lane, d + i * 4096 + wi * 1024);
        else glds16(vg + (size_t)st * 8192 + (i - 2) * 128, v_lane, d + 8192 + wi * 2048 + (i - 2) * 1024);
    };
    bf16x8 Q[NB][4];
    {
        const int xr = lane >> 2, xpos = lane & 3;
        const unsigned x_lane = (unsigned)(xr * (DM * 2) + ((xpos ^ ((4 - (xr >> 2)) & 3)) * 16));
        const unsigned wq_lane = (unsigned)((16 * (wi & 1) + 8 * (tr_row >> 2) + 4 * (wi >> 1) + (tr_row & 3)) * (NH * DH * 2) + tr_colB_of(lane));
        const int xb2 = (w < 5) ? 8 + w : 12;
        const char* xs1 = xg + (size_t)(w * 16) * (DM * 2);
        const char* xs2 = xg + (size_t)(xb2 * 16) * (DM * 2);
        const unsigned xd1 = (unsigned)(X_RING + w * 1024), xd2 = (unsigned)(X_RING + xb2 * 1024);
        const unsigned wd = (unsigned)(W_RING + grp * 8192 + wi * 2048);
        auto issue_xw = [&](int ks) {
            const int slot = ks % 3;
            glds16(xs1 + ks * 64, x_lane, xd1 + slot * X_SLOT);
            glds16(wqg + (size_t)ks * 32 * (NH * DH * 2), wq_lane, wd + slot * W_SLOT);
            glds16(wqg + (size_t)ks * 32 * (NH * DH * 2) + 128, wq_lane, wd + slot * W_SLOT + 1024);
            glds16(xs2 + ks * 64, x_lane, xd2 + slot * X_SLOT);
        };
        f32x4 acc[NB][8];
#pragma unroll
        for (int nb = 0; nb < NB; ++nb)
#pragma unroll
            for (int t = 0; t < 8; ++t) acc[nb][t] = (f32x4){0.f, 0.f, 0.f, 0.f};
        issue_xw(0);
        issue_xw(1);
        const unsigned xb = (unsigned)(X_RING + rb0 * 1024 + m * 64 + ((g ^ ((4 - q4) & 3)) * 16));
        const unsigned wb = (unsigned)(W_RING + grp * 8192 + hh * 2048 + (4 * g1 + q4) * 64 + ((16 * p4) ^ (32 * g1)));
#pragma unroll
        for (int ks = 0; ks < 16; ++ks) {
            if (ks == 0) wait_bar<4>(); else if (ks <= 8) wait_bar<5>(); else if (ks < 15) wait_bar<4>(); else wait_bar<0>();
            if (ks < 14) issue_xw(ks + 2);
            if (ks < 8) kv_one(ks >> 2, ks & 3);
            const int slot = ks % 3;
            bf16x8 xf[NB];
#pragma unroll
            for (int nb = 0; nb < NB; ++nb) xf[nb] = lds_b128(xb + slot * X_SLOT + nb * 1024);
#pragma unroll
            for (int t = 0; t < 8; ++t) {
                const int c = t >> 1, u = t & 1;
                const bf16x8 a = cat44(lds_tr(wb + slot * W_SLOT + c * 512 + u * 8), lds_tr(wb + slot * W_SLOT + 4096 + c * 512 + u * 8));
#pragma unroll
                for (int nb = 0; nb < NB; ++nb) acc[nb][t] = MFMA16(a, xf[nb], acc[nb][t]);
            }
        }
#pragma unroll
        for (int nb = 0; nb < NB; ++nb)
#pragma unroll
            for (int c = 0; c < 4; ++c) Q[nb][c] = pack8(acc[nb][2 * c] * QSCALE, acc[nb][2 * c + 1] * QSCALE);
    }

    f32x4 O[NB][8];
    float negm[NB], l[NB];
#pragma unroll
    for (int nb = 0; nb < NB; ++nb) {
        negm[nb] = 0.f; l[nb] = 0.f;
#pragma unroll
        for (int t = 0; t < 8; ++t) O[nb][t] = (f32x4){0.f, 0.f, 0.f, 0.f};
    }
    const int l2 = opaque_lane();
    const int m2 = l2 & 15, g2 = l2 >> 4;
    {
        const int kk2 = 8 * wi + (l2 >> 3);
        k_lane = (unsigned)(kk2 * 256 + (((l2 & 7) ^ ((kk2 >> 1) & 7)) * 16));
        v_lane = (unsigned)((8 * wi + ((l2 & 31) >> 2)) * 256 + tr_colB_of(l2));
    }
    unsigned kE = (unsigned)(grp * 16384 + m2 * 128 + ((g2 ^ (m2 >> 1)) * 16));
    unsigned vE = (unsigned)(grp * 16384 + 8192 + (l2 >> 5) * 2048 + (4 * (g2 & 1) + (m2 >> 2)) * 64 + 32 * (g2 & 1) + 8 * (l2 & 3));
    wait_bar<0>();
#pragma unroll
    for (int i = 0; i < 4; ++i) kv_one(2, i);
#pragma unroll
    for (int i = 0; i < 4; ++i) kv_one(3, i);
    constexpr int NQR = (NB == 4) ? 3 : NB;
    const unsigned q4a = (unsigned)(4 * KV_STAGE + grp * 4096 + l2 * 16);
    if (NB == 4) {
#pragma unroll
        for (int c = 0; c < 4; ++c) *(LDS_AS bf16x8*)(q4a + c * 1024) = Q[NB - 1][c];
    }
    float lo = -INFINITY;
#pragma clang loop unroll(disable)
    for (int j = 0; j < 16; ++j) {
        if (j > 0) { if (j < 15) { WAIT_BAR(8); } else { WAIT_BAR(4); } }
        if (j >= 1 && j < 13) {
#pragma unroll
            for (int i = 0; i < 4; ++i) kv_one(j + 3, i);
        } else if (j == 13) {
            const int ln = opaque_lane();
            const unsigned wo_lane = (unsigned)(((ln & 31) >> 2) * (DM * 2) + tr_colB_of(ln));
#pragma unroll
            for (int i = 0; i < 4; ++i) glds16(wog + (size_t)(8 * i) * (DM * 2), wo_lane, (unsigned)(w * 4096 + i * 1024));
        }
        f32x4 S[NB][2];
#pragma unroll
        for (int nb = 0; nb < NB; ++nb) { S[nb][0] = (f32x4){negm[nb], negm[nb], negm[nb], negm[nb]}; S[nb][1] = S[nb][0]; }
#pragma unroll
        for (int c = 0; c < 4; ++c) {
            bf16x8 q3;
            if (NB == 4) q3 = lds_b128(q4a + c * 1024);
#pragma unroll
            for (int kt = 0; kt < 2; ++kt) {
                const bf16x8 a = lds_b128(((c & 1) ? (kE ^ 64u) : kE) + (c >> 1) * 4096 + kt * 2048);
#pragma unroll
                for (int nb = 0; nb < NQR; ++nb) S[nb][kt] = MFMA16(a, Q[nb][c], S[nb][kt]);
                if (NB == 4) S[NB - 1][kt] = MFMA16(a, q3, S[NB - 1][kt]);
            }
        }
        float mx[NB];
#pragma unroll
        for (int nb = 0; nb < NB; ++nb) {
            float a = max3f(S[nb][0][0], S[nb][0][1], S[nb][0][2]);
            a = max3f(a, S[nb][0][3], S[nb][1][0]);
            a = max3f(a, S[nb][1][1], S[nb][1][2]);
            mx[nb] = __builtin_fmaxf(a, S[nb][1][3]);
        }
        float mxa = mx[0];
#pragma unroll
        for (int nb = 1; nb < NB; ++nb) mxa = __builtin_fmaxf(mxa, mx[nb]);
        if (j == 0 || __any(mxa > THR)) {
#pragma unroll
            for (int nb = 0; nb < NB; ++nb) {
                float rm = mx[nb];
                rm = __builtin_fmaxf(rm, __shfl_xor(rm, 16));
                rm = __builtin_fmaxf(rm, __shfl_xor(rm, 32));
                const float delta = __builtin_fmaxf(rm, lo);
                const float alpha = __builtin_amdgcn_exp2f(__builtin_fminf(-delta, 0.f));
                negm[nb] -= delta; l[nb] *= alpha;
                S[nb][0] -= delta; S[nb][1] -= delta;
#pragma unroll
                for (int t = 0; t < 8; ++t) O[nb][t] *= alpha;
            }
            lo = 0.f;
        }
        bf16x8 P[NB];
#pragma unroll
        for (int nb = 0; nb < NB; ++nb) {
            f32x4 e0, e1;
#pragma unroll
            for (int r = 0; r < 4; ++r) { e0[r] = __builtin_amdgcn_exp2f(S[nb][0][r]); e1[r] = __builtin_amdgcn_exp2f(S[nb][1][r]); }
            l[nb] += ((e0[0] + e0[1]) + (e0[2] + e0[3])) + ((e1[0] + e1[1]) + (e1[2] + e1[3]));
            P[nb] = pack8(e0, e1);
        }
#pragma unroll
        for (int dt = 0; dt < 8; ++dt) {
            const unsigned vb = ((dt & 1) ? (vE ^ 32u) : vE) + (dt >> 1) * 512;
            const bf16x8 a = cat44(lds_tr(vb), lds_tr(vb + 4096));
#pragma unroll
            for (int nb = 0; nb < NB; ++nb) O[nb][dt] = MFMA16(a, P[nb], O[nb][dt]);
        }
        kE = (kE + KV_STAGE) & (4 * KV_STAGE - 1);
        vE = (vE + KV_STAGE) & (4 * KV_STAGE - 1);
    }
    const int ln = opaque_lane();
#pragma unroll
    for (int nb = 0; nb < NB; ++nb) {
        float s = l[nb];
        s += __shfl_xor(s, 16);
        s += __shfl_xor(s, 32);
        const float inv = 1.0f / s;
        char* dst = ofp + (size_t)(rb0 + nb) * (128 * 1024) + ln * 16;
#pragma unroll
        for (int c = 0; c < 4; ++c) *(bf16x8*)(dst + c * 1024) = pack8(O[nb][2 * c] * inv, O[nb][2 * c + 1] * inv);
    }
    asm volatile("s_waitcnt vmcnt(0)" ::: "memory");
    {
        const int G1 = (ln >> 4) & 1, Q4 = (ln & 15) >> 2;
        const unsigned bE = (unsigned)(w * 4096 + (ln >> 5) * 1024 + (4 * G1 + Q4) * 64 + 32 * G1 + 8 * (ln & 3));
#pragma unroll
        for (int nt = 0; nt < 4; ++nt) {
            const unsigned b = ((nt & 1) ? (bE ^ 32u) : bE) + (nt >> 1) * 512;
            const bf16x8 a = cat44(lds_tr(b), lds_tr(b + 2048));
            *(bf16x8*)(wfp + nt * 1024 + ln * 16) = a;
        }
    }
}

__global__ __launch_bounds__(512, 2) void attn_k(Ptrs p, char* __restrict__ ws) {
    const int bid = blockIdx.x, xcd = bid & 7, idx = bid >> 3;
    const int inst = xcd >> 2, hp = (xcd & 3) * 4 + (idx & 3), b = idx >> 2;
    const int tid = threadIdx.x;
    const int w = __builtin_amdgcn_readfirstlane(tid >> 6);
    const int grp = w >> 2, wi = w & 3, head = hp * 2 + grp;
    const char* xg = (const char*)(p.x[inst]) + (size_t)b * SQ * DM * 2;
    const char* wqg = (const char*)(p.wq[inst]) + (size_t)head * DH * 2;
    const char* kg = (const char*)(p.k[inst]) + (size_t)(b * NH + head) * SK * DH * 2;
    const char* vg = (const char*)(p.v[inst]) + (size_t)(b * NH + head) * SK * DH * 2;
    const char* wog = (const char*)(p.wo[inst]) + ((size_t)(hp * 256 + w * 32) * DM + b * 64) * 2;
    char* ofp = ws + WS_OF + ((size_t)(inst * 8 + b) * NRB * 128 + head * 4) * 1024;
    char* wfp = ws + WS_WF + (((size_t)inst * 128 + hp * 8 + w) * 32 + b * 4) * 1024;
    const bool heavy = grp ? (wi == 3) : (wi == 0);
    const int rb0 = grp ? 3 * wi : (wi == 0 ? 0 : 3 * wi + 1);
    if (heavy) attn_wave<4>(xg, wqg, kg, vg, wog, ofp, wfp, w, grp, wi, rb0);
    else attn_wave<3>(xg, wqg, kg, vg, wog, ofp, wfp, w, grp, wi, rb0);
}

template <int NB>
__device__ __forceinline__ void oproj_body(const char* __restrict__ of, const char* __restrict__ wf, __hip_bfloat16* __restrict__ outp,
                                           char* smem, int w, int lane) {
    f32x4 acc[NB][4];
#pragma unroll
    for (int nb = 0; nb < NB; ++nb)
#pragma unroll
        for (int t = 0; t < 4; ++t) acc[nb][t] = (f32x4){0.f, 0.f, 0.f, 0.f};
    const char* op = of + (size_t)(w * 16) * 1024 + lane * 16;
    const char* wp = wf + (size_t)(w * 16) * (32 * 1024) + lane * 16;
    bf16x8 o0[NB], w0[4], o1[NB], w1[4];
    auto load = [&](bf16x8* o, bf16x8* wv, int kc) {
#pragma unroll
        for (int t = 0; t < 4; ++t) wv[t] = *(const bf16x8*)(wp + (size_t)kc * (32 * 1024) + t * 1024);
#pragma unroll
        for (int nb = 0; nb < NB; ++nb) o[nb] = *(const bf16x8*)(op + (size_t)nb * (128 * 1024) + kc * 1024);
    };
    auto comp = [&](const bf16x8* o, const bf16x8* wv) {
#pragma unroll
        for (int nb = 0; nb < NB; ++nb)
#pragma unroll
            for (int t = 0; t < 4; ++t) acc[nb][t] = MFMA16(wv[t], o[nb], acc[nb][t]);
    };
    load(o0, w0, 0);
#pragma unroll 1
    for (int kc = 0; kc < 16; kc += 2) {
        load(o1, w1, kc + 1);
        comp(o0, w0);
        if (kc + 2 < 16) load(o0, w0, kc + 2);
        comp(o1, w1);
    }
    f32x4* red = (f32x4*)smem;
    constexpr int NT = NB * 4;
#pragma unroll
    for (int r0 = 0; r0 < NT; r0 += 16) {
        if (r0) __syncthreads();
#pragma unroll
        for (int T = r0; T < (r0 + 16 < NT ? r0 + 16 : NT); ++T) red[((T - r0) * 8 + w) * 64 + lane] = acc[T >> 2][T & 3];
        __syncthreads();
#pragma unroll
        for (int h = 0; h < 2; ++h) {
            const int T = r0 + h * 8 + w;
            if (T < NT) {
                f32x4 s = red[((T - r0) * 8 + 0) * 64 + lane];
#pragma unroll
                for (int k = 1; k < 8; ++k) s += red[((T - r0) * 8 + k) * 64 + lane];
                const int row = (T >> 2) * 16 + (lane & 15), col = (T & 3) * 16 + (lane >> 4) * 4;
                u32x2 o = {cvtpk(s[0], s[1]), cvtpk(s[2], s[3])};
                *(u32x2*)(outp + (size_t)row * 1024 + col) = o;
            }
        }
    }
}

__global__ __launch_bounds__(512, 2) void oproj_k(const char* __restrict__ ws, __hip_bfloat16* __restrict__ out) {
    __shared__ __attribute__((aligned(16))) char smem[16 * 8 * 1024];
    const int bid = blockIdx.x, xcd = bid & 7, idx = bid >> 3;
    const int ib = 2 * xcd + (idx >> 4), half = (idx >> 3) & 1, nt = idx & 7;
    const int inst = ib >> 3, b = ib & 7;
    const int lane = threadIdx.x & 63;
    const int w = __builtin_amdgcn_readfirstlane(threadIdx.x >> 6);
    const int rb0 = half ? 7 : 0;
    const char* of = ws + WS_OF + ((size_t)ib * NRB + rb0) * (128 * 1024);
    const char* wf = ws + WS_WF + ((size_t)inst * 128 * 32 + nt * 4) * 1024;
    __hip_bfloat16* outp = out + (size_t)(b * SQ + rb0 * 16) * 1024 + inst * 512 + nt * 64;
    if (half) oproj_body<6>(of, wf, outp, smem, w, lane);
    else oproj_body<7>(of, wf, outp, smem, w, lane);
}

extern "C" void kernel_launch(void* const* d_in, const int* in_sizes, int n_in, void* d_out, int out_size, void* d_ws,
                              size_t ws_size, hipStream_t stream) {
    Ptrs p;
    for (int i = 0; i < 2; ++i) {
        p.x[i] = (const __hip_bfloat16*)d_in[5 * i + 0];
        p.k[i] = (const __hip_bfloat16*)d_in[5 * i + 1];
        p.v[i] = (const __hip_bfloat16*)d_in[5 * i + 2];
        p.wq[i] = (const __hip_bfloat16*)d_in[5 * i + 3];
        p.wo[i] = (const __hip_bfloat16*)d_in[5 * i + 4];
    }
    (void)hipFuncSetAttribute((const void*)attn_k, hipFuncAttributeMaxDynamicSharedMemorySize, LDS1);
    hipLaunchKernelGGL(attn_k, dim3(256), dim3(512), LDS1, stream, p, (char*)d_ws);
    hipLaunchKernelGGL(oproj_k, dim3(256), dim3(512), 0, stream, (const char*)d_ws, (__hip_bfloat16*)d_out);
}
```

```cpp
#include <hip/hip_runtime.h>
#include <hip/hip_bf16.h>

typedef __attribute__((ext_vector_type(8))) short bf16x8;
typedef __attribute__((ext_vector_type(4))) short s16x4;
typedef __attribute__((ext_vector_type(4))) float f32x4;
typedef __attribute__((ext_vector_type(2))) float f32x2;
typedef __attribute__((ext_vector_type(4))) unsigned u32x4;
typedef __attribute__((ext_vector_type(2))) unsigned u32x2;
typedef __attribute__((ext_vector_type(2))) __bf16 bf16x2_t;
typedef short v4i16_t __attribute__((ext_vector_type(4)));
#define LDS_AS __attribute__((address_space(3)))

constexpr int NB_BATCH = 8, SQ = 208, DM = 512, NH = 32, SK = 512, DH = 128;
constexpr int NRB = 13;
constexpr float QSCALE = 0.08838834764831845f * 1.4426950408889634f;
constexpr float LIM = 1024.0f;

constexpr int KV_STAGE = 32768;
constexpr int X_SLOT = 13 * 1024, W_SLOT = 16384;
constexpr int X_RING = 65536, W_RING = X_RING + 3 * X_SLOT;
constexpr int LDS1 = 4 * KV_STAGE + 16384 + 8192;

constexpr size_t WS_OF = 0;
constexpr size_t OF_BYTES = (size_t)16 * NRB * 128 * 1024;
constexpr size_t WS_WF = OF_BYTES;
constexpr size_t WF_BYTES = (size_t)2 * 128 * 32 * 1024;

#define STAMP(i)
#define STAMP_ARG
#define MFMA16(a, b, c) __builtin_amdgcn_mfma_f32_16x16x32_bf16(a, b, c, 0, 0, 0)
#define WAIT_BAR(N) asm volatile("s_waitcnt vmcnt(" #N ") lgkmcnt(0)\n\ts_barrier" ::: "memory")

__device__ __forceinline__ void glds16(const char* sbase, unsigned voff, unsigned lds_base) {
    unsigned sv;
    asm volatile("s_mov_b32 %0, m0\n\ts_mov_b32 m0, %3\n\ts_nop 0\n\tglobal_load_lds_dwordx4 %1, %2\n\ts_mov_b32 m0, %0"
                 : "=&s"(sv) : "v"(voff), "s"(sbase), "s"(lds_base) : "memory");
}
__device__ __forceinline__ bf16x8 lds_b128(unsigned a) { return *(const LDS_AS bf16x8*)(a); }
__device__ __forceinline__ s16x4 lds_tr(unsigned a) {
    return __builtin_bit_cast(s16x4, __builtin_amdgcn_ds_read_tr16_b64_v4i16((LDS_AS v4i16_t*)(a)));
}
__device__ __forceinline__ bf16x8 cat44(s16x4 l, s16x4 h) { return (bf16x8){l[0], l[1], l[2], l[3], h[0], h[1], h[2], h[3]}; }
__device__ __forceinline__ unsigned cvtpk(float lo, float hi) {
    f32x2 v = {lo, hi};
    return __builtin_bit_cast(unsigned, __builtin_convertvector(v, bf16x2_t));
}
__device__ __forceinline__ bf16x8 pack8(f32x4 a, f32x4 b) {
    u32x4 w = {cvtpk(a[0], a[1]), cvtpk(a[2], a[3]), cvtpk(b[0], b[1]), cvtpk(b[2], b[3])};
    return __builtin_bit_cast(bf16x8, w);
}
__device__ __forceinline__ float max3f(float a, float b, float c) { return __builtin_fmaxf(__builtin_fmaxf(a, b), c); }

struct Ptrs {
    const __hip_bfloat16* x[2];
    const __hip_bfloat16* k[2];
    const __hip_bfloat16* v[2];
    const __hip_bfloat16* wq[2];
    const __hip_bfloat16* wo[2];
};

__device__ __forceinline__ int opaque_lane() {
    int l;
    asm volatile("v_mbcnt_lo_u32_b32 %0, -1, 0\n\tv_mbcnt_hi_u32_b32 %0, -1, %0" : "=v"(l));
    return l;
}
template <int N> __device__ __forceinline__ void wait_bar() {
    asm volatile("s_waitcnt vmcnt(%0) lgkmcnt(0)\n\ts_barrier" :: "n"(N) : "memory");
}
__device__ __forceinline__ int tr_colB_of(int lane) {
    const int tr_row = (lane & 31) >> 2, cpos = lane & 3;
    return (lane >> 5) * 64 + (((cpos >> 1) ^ (tr_row >> 2)) * 32) + (cpos & 1) * 16;
}

__device__ __forceinline__ float max2f(float a, float b) { return __builtin_amdgcn_fmed3f(a, b, INFINITY); }

__global__ __launch_bounds__(512, 2) void attn_k(Ptrs p, char* __restrict__ ws) {
    const int bid = blockIdx.x, xcd = bid & 7, idx = bid >> 3;
    const int inst = xcd >> 2, hp = (xcd & 3) * 4 + (idx & 3), b = idx >> 2;
    const int w = __builtin_amdgcn_readfirstlane((int)threadIdx.x >> 6);
    const int grp = w >> 2, wi = w & 3, head = hp * 2 + grp;
    const char* xg = (const char*)(p.x[inst]) + (size_t)b * SQ * DM * 2;
    const char* wqg = (const char*)(p.wq[inst]) + (size_t)head * DH * 2;
    const char* kg = (const char*)(p.k[inst]) + (size_t)(b * NH + head) * SK * DH * 2;
    const char* vg = (const char*)(p.v[inst]) + (size_t)(b * NH + head) * SK * DH * 2;
    const char* wog = (const char*)(p.wo[inst]) + ((size_t)(hp * 256 + w * 32) * DM + b * 64) * 2;
    char* ofp = ws + WS_OF + ((size_t)(inst * 8 + b) * NRB * 128 + head * 4) * 1024;
    char* wfp = ws + WS_WF + (((size_t)inst * 128 + hp * 8 + w) * 32 + b * 4) * 1024;
    const int rb0 = 3 * wi;
    const int rot = (int)(idx * 5 + xcd * 3) & 15;
    const int lane = threadIdx.x & 63;
    const int m = lane & 15, g = lane >> 4, q4 = m >> 2, p4 = lane & 3, hh = lane >> 5, g1 = g & 1;
    STAMP(0);
    const int tr_row = (lane & 31) >> 2;
    const int kkey = 8 * wi + (lane >> 3);
    unsigned k_lane = (unsigned)(kkey * 256 + (((lane & 7) ^ ((kkey >> 1) & 7)) * 16));
    unsigned v_lane = (unsigned)((8 * wi + tr_row) * 256 + tr_colB_of(lane));
    auto kv_one = [&](int st, int i) {
        const unsigned d = (unsigned)((st & 3) * KV_STAGE + grp * 16384);
        const int sg = (st + rot) & 15;
        if (i < 2) glds16(kg + (size_t)sg * 8192 + i * 128, k_lane, d + i * 4096 + wi * 1024);
        else glds16(vg + (size_t)sg * 8192 + (i - 2) * 128, v_lane, d + 8192 + wi * 2048 + (i - 2) * 1024);
    };
    bf16x8 Q[3][4];
    bf16x8 q12c;
    {
        const int xr = lane >> 2, xpos = lane & 3;
        const unsigned x_lane = (unsigned)(xr * (DM * 2) + ((xpos ^ ((4 - (xr >> 2)) & 3)) * 16));
        const unsigned wq_lane = (unsigned)((16 * (wi & 1) + 8 * (tr_row >> 2) + 4 * (wi >> 1) + (tr_row & 3)) * (NH * DH * 2) + tr_colB_of(lane));
        const int xb2 = (w < 5) ? 8 + w : 12;
        const char* xs1 = xg + (size_t)(w * 16) * (DM * 2);
        const char* xs2 = xg + (size_t)(xb2 * 16) * (DM * 2);
        const unsigned xd1 = (unsigned)(X_RING + w * 1024), xd2 = (unsigned)(X_RING + xb2 * 1024);
        const unsigned wd = (unsigned)(W_RING + grp * 8192 + wi * 2048);
        auto issue_xw = [&](int ks) {
            const int slot = ks % 3;
            glds16(xs1 + ks * 64, x_lane, xd1 + slot * X_SLOT);
            glds16(wqg + (size_t)ks * 32 * (NH * DH * 2), wq_lane, wd + slot * W_SLOT);
            glds16(wqg + (size_t)ks * 32 * (NH * DH * 2) + 128, wq_lane, wd + slot * W_SLOT + 1024);
            glds16(xs2 + ks * 64, x_lane, xd2 + slot * X_SLOT);
        };
        f32x4 acc[3][8], acc12[2];
#pragma unroll
        for (int nb = 0; nb < 3; ++nb)
#pragma unroll
            for (int t = 0; t < 8; ++t) acc[nb][t] = (f32x4){0.f, 0.f, 0.f, 0.f};
        acc12[0] = acc12[1] = (f32x4){0.f, 0.f, 0.f, 0.f};
        issue_xw(0);
        issue_xw(1);
        const unsigned xl = (unsigned)(X_RING + m * 64 + ((g ^ ((4 - q4) & 3)) * 16));
        const unsigned xb = xl + rb0 * 1024;
        const unsigned wb = (unsigned)(W_RING + grp * 8192 + hh * 2048 + (4 * g1 + q4) * 64 + ((16 * p4) ^ (32 * g1)));
        const unsigned wb12 = wb + wi * 512;
#pragma unroll
        for (int ks = 0; ks < 16; ++ks) {
            if (ks == 0) wait_bar<4>(); else if (ks <= 8) wait_bar<5>(); else if (ks < 15) wait_bar<4>(); else wait_bar<0>();
            if (ks < 14) issue_xw(ks + 2);
            if (ks < 8) kv_one(ks >> 2, ks & 3);
            const int slot = ks % 3;
            bf16x8 xf[3];
#pragma unroll
            for (int nb = 0; nb < 3; ++nb) xf[nb] = lds_b128(xb + slot * X_SLOT + nb * 1024);
            const bf16x8 x12 = lds_b128(xl + slot * X_SLOT + 12 * 1024);
#pragma unroll
            for (int t = 0; t < 8; ++t) {
                const int c = t >> 1, u = t & 1;
                const bf16x8 a = cat44(lds_tr(wb + slot * W_SLOT + c * 512 + u * 8), lds_tr(wb + slot * W_SLOT + 4096 + c * 512 + u * 8));
#pragma unroll
                for (int nb = 0; nb < 3; ++nb) acc[nb][t] = MFMA16(a, xf[nb], acc[nb][t]);
            }
#pragma unroll
            for (int u = 0; u < 2; ++u) {
                const bf16x8 a = cat44(lds_tr(wb12 + slot * W_SLOT + u * 8), lds_tr(wb12 + slot * W_SLOT + 4096 + u * 8));
                acc12[u] = MFMA16(a, x12, acc12[u]);
            }
        }
#pragma unroll
        for (int nb = 0; nb < 3; ++nb)
#pragma unroll
            for (int c = 0; c < 4; ++c) Q[nb][c] = pack8(acc[nb][2 * c] * QSCALE, acc[nb][2 * c + 1] * QSCALE);
        q12c = pack8(acc12[0] * QSCALE, acc12[1] * QSCALE);
    }

    f32x4 O[3][8], O12[2];
    f32x4 negm[4];
    float l[4];
#pragma unroll
    for (int nb = 0; nb < 4; ++nb) { negm[nb] = (f32x4){0.f, 0.f, 0.f, 0.f}; l[nb] = 0.f; }
#pragma unroll
    for (int nb = 0; nb < 3; ++nb)
#pragma unroll
        for (int t = 0; t < 8; ++t) O[nb][t] = (f32x4){0.f, 0.f, 0.f, 0.f};
    O12[0] = O12[1] = (f32x4){0.f, 0.f, 0.f, 0.f};
    const int l2 = opaque_lane();
    const int m2 = l2 & 15, g2 = l2 >> 4;
    {
        const int kk2 = 8 * wi + (l2 >> 3);
        k_lane = (unsigned)(kk2 * 256 + (((l2 & 7) ^ ((kk2 >> 1) & 7)) * 16));
        v_lane = (unsigned)((8 * wi + ((l2 & 31) >> 2)) * 256 + tr_colB_of(l2));
    }
    unsigned kE = (unsigned)(grp * 16384 + m2 * 128 + ((g2 ^ (m2 >> 1)) * 16));
    unsigned vE = (unsigned)(grp * 16384 + 8192 + (l2 >> 5) * 2048 + (4 * (g2 & 1) + (m2 >> 2)) * 64 + 32 * (g2 & 1) + 8 * (l2 & 3));
    const unsigned q4a = (unsigned)(4 * KV_STAGE + 16384 + grp * 4096 + l2 * 16);
    *(LDS_AS bf16x8*)(q4a + wi * 1024) = q12c;
    STAMP(1);
#define SB() __builtin_amdgcn_sched_barrier(0)
    WAIT_BAR(0);
    STAMP(2);
#pragma unroll
    for (int i = 0; i < 4; ++i) kv_one(2, i);
    auto sync = [&](int j) {
        WAIT_BAR(4);
        if (j < 13) {
#pragma unroll
            for (int i = 0; i < 4; ++i) kv_one(j + 3, i);
        } else if (j == 13) {
            const int ln = opaque_lane();
            const unsigned wo_lane = (unsigned)(((ln & 31) >> 2) * (DM * 2) + tr_colB_of(ln));
#pragma unroll
            for (int i = 0; i < 4; ++i) glds16(wog + (size_t)(8 * i) * (DM * 2), wo_lane, (unsigned)(w * 4096 + i * 1024));
        }
    };
    auto ldk = [&](int c, int kt) { return lds_b128(((c & 1) ? (kE ^ 64u) : kE) + (c >> 1) * 4096 + kt * 2048); };
    auto ldv = [&](int dt) {
        const unsigned vb = ((dt & 1) ? (vE ^ 32u) : vE) + (dt >> 1) * 512;
        return cat44(lds_tr(vb), lds_tr(vb + 4096));
    };
#define PRIO 0
    if (PRIO == 1 && grp == 1) __builtin_amdgcn_s_setprio(1);
    if (PRIO == 2 && grp == 0) __builtin_amdgcn_s_setprio(1);
    float lo = -INFINITY;
    bf16x8 kf0 = ldk(0, 0), kf1 = ldk(0, 1), qf = lds_b128(q4a);
#pragma clang loop unroll(disable)
    for (int j = 0; j < 16; ++j) {
        STAMP(8 + j);
        f32x4 S[4][2];
#pragma unroll
        for (int c = 0; c < 4; ++c) {
            bf16x8 n0, n1, nq;
            if (c < 3) { n0 = ldk(c + 1, 0); nq = lds_b128(q4a + (c + 1) * 1024); n1 = ldk(c + 1, 1); }
            SB();
#pragma unroll
            for (int nb = 0; nb < 3; ++nb) S[nb][0] = MFMA16(kf0, Q[nb][c], c == 0 ? negm[nb] : S[nb][0]);
            S[3][0] = MFMA16(kf0, qf, c == 0 ? negm[3] : S[3][0]);
#pragma unroll
            for (int nb = 0; nb < 3; ++nb) S[nb][1] = MFMA16(kf1, Q[nb][c], c == 0 ? negm[nb] : S[nb][1]);
            S[3][1] = MFMA16(kf1, qf, c == 0 ? negm[3] : S[3][1]);
            SB();
            if (c < 3) { kf0 = n0; kf1 = n1; qf = nq; }
        }
        if (grp == 0 && j < 15) sync(j);
        bf16x8 vf[2];
#pragma unroll
        for (int dt = 0; dt < 2; ++dt) vf[dt] = ldv(dt);
        SB();
        bf16x8 P[4];
        float ps[4];
        auto softmax_tile = [&]() {
#pragma unroll
            for (int nb = 0; nb < 4; ++nb) {
                f32x4 e0, e1;
#pragma unroll
                for (int r = 0; r < 4; ++r) { e0[r] = __builtin_amdgcn_exp2f(S[nb][0][r]); e1[r] = __builtin_amdgcn_exp2f(S[nb][1][r]); }
                ps[nb] = ((e0[0] + e0[1]) + (e0[2] + e0[3])) + ((e1[0] + e1[1]) + (e1[2] + e1[3]));
                P[nb] = pack8(e0, e1);
            }
        };
        softmax_tile();
        const float psm = max3f(max2f(ps[0], ps[1]), ps[2], ps[3]);
        if (j == 0 || __any(!(psm <= LIM))) {
#pragma unroll
            for (int nb = 0; nb < 4; ++nb) {
                float rm = max2f(S[nb][0][0], S[nb][0][1]);
                rm = max3f(rm, S[nb][0][2], S[nb][0][3]);
                rm = max3f(rm, S[nb][1][0], S[nb][1][1]);
                rm = max3f(rm, S[nb][1][2], S[nb][1][3]);
                rm = __builtin_fmaxf(rm, __shfl_xor(rm, 16));
                rm = __builtin_fmaxf(rm, __shfl_xor(rm, 32));
                const float delta = __builtin_fmaxf(rm, lo);
                const float alpha = __builtin_amdgcn_exp2f(__builtin_fminf(-delta, 0.f));
                negm[nb] -= delta; l[nb] *= alpha;
                S[nb][0] -= delta; S[nb][1] -= delta;
                if (nb < 3) {
#pragma unroll
                    for (int t = 0; t < 8; ++t) O[nb][t] *= alpha;
                } else { O12[0] *= alpha; O12[1] *= alpha; }
            }
            lo = 0.f;
            softmax_tile();
        }
#pragma unroll
        for (int nb = 0; nb < 4; ++nb) l[nb] += ps[nb];
        if (grp == 1 && j < 15) sync(j);
        kE = (kE + KV_STAGE) & (4 * KV_STAGE - 1);
        {
            bf16x8 va[3], vb3[3];
#pragma unroll
            for (int dt = 0; dt < 3; ++dt) va[dt] = ldv(2 + dt);
            SB();
#pragma unroll
            for (int dt = 0; dt < 2; ++dt)
#pragma unroll
                for (int nb = 0; nb < 3; ++nb) O[nb][dt] = MFMA16(vf[dt], P[nb], O[nb][dt]);
            SB();
#pragma unroll
            for (int dt = 0; dt < 3; ++dt) vb3[dt] = ldv(5 + dt);
            SB();
#pragma unroll
            for (int dt = 0; dt < 3; ++dt)
#pragma unroll
                for (int nb = 0; nb < 3; ++nb) O[nb][2 + dt] = MFMA16(va[dt], P[nb], O[nb][2 + dt]);
            SB();
            const unsigned vb = vE + wi * 512;
            const bf16x8 v12a = cat44(lds_tr(vb), lds_tr(vb + 4096)), v12b = cat44(lds_tr(vb ^ 32u), lds_tr((vb ^ 32u) + 4096));
            kf0 = ldk(0, 0); qf = lds_b128(q4a); kf1 = ldk(0, 1);
            SB();
#pragma unroll
            for (int dt = 0; dt < 3; ++dt)
#pragma unroll
                for (int nb = 0; nb < 3; ++nb) O[nb][5 + dt] = MFMA16(vb3[dt], P[nb], O[nb][5 + dt]);
            O12[0] = MFMA16(v12a, P[3], O12[0]);
            O12[1] = MFMA16(v12b, P[3], O12[1]);
            SB();
        }
        vE = (vE + KV_STAGE) & (4 * KV_STAGE - 1);
    }
    STAMP(3);
    const int ln = opaque_lane();
#pragma unroll
    for (int nb = 0; nb < 4; ++nb) {
        float s = l[nb];
        s += __shfl_xor(s, 16);
        s += __shfl_xor(s, 32);
        const float inv = 1.0f / s;
        if (nb < 3) {
            char* dst = ofp + (size_t)(rb0 + nb) * (128 * 1024) + ln * 16;
#pragma unroll
            for (int c = 0; c < 4; ++c) *(bf16x8*)(dst + c * 1024) = pack8(O[nb][2 * c] * inv, O[nb][2 * c + 1] * inv);
        } else {
            *(bf16x8*)(ofp + (size_t)12 * (128 * 1024) + wi * 1024 + ln * 16) = pack8(O12[0] * inv, O12[1] * inv);
        }
    }
    asm volatile("s_waitcnt vmcnt(0)" ::: "memory");
    {
        const int G1 = (ln >> 4) & 1, Q4 = (ln & 15) >> 2;
        const unsigned bE = (unsigned)(w * 4096 + (ln >> 5) * 1024 + (4 * G1 + Q4) * 64 + 32 * G1 + 8 * (ln & 3));
#pragma unroll
        for (int nt = 0; nt < 4; ++nt) {
            const unsigned bb = ((nt & 1) ? (bE ^ 32u) : bE) + (nt >> 1) * 512;
            const bf16x8 a = cat44(lds_tr(bb), lds_tr(bb + 2048));
            *(bf16x8*)(wfp + nt * 1024 + ln * 16) = a;
        }
    }
    STAMP(4);
}

template <int NB>
__device__ __forceinline__ void oproj_body(const char* __restrict__ of, const char* __restrict__ wf, __hip_bfloat16* __restrict__ outp,
                                           char* smem, int w, int lane) {
    f32x4 acc[NB][4];
#pragma unroll
    for (int nb = 0; nb < NB; ++nb)
#pragma unroll
        for (int t = 0; t < 4; ++t) acc[nb][t] = (f32x4){0.f, 0.f, 0.f, 0.f};
    const char* op = of + (size_t)(w * 16) * 1024 + lane * 16;
    const char* wp = wf + (size_t)(w * 16) * (32 * 1024) + lane * 16;
    bf16x8 o0[NB], w0[4], o1[NB], w1[4];
    auto load = [&](bf16x8* o, bf16x8* wv, int kc) {
#pragma unroll
        for (int t = 0; t < 4; ++t) wv[t] = *(const bf16x8*)(wp + (size_t)kc * (32 * 1024) + t * 1024);
#pragma unroll
        for (int nb = 0; nb < NB; ++nb) o[nb] = *(const bf16x8*)(op + (size_t)nb * (128 * 1024) + kc * 1024);
    };
    auto comp = [&](const bf16x8* o, const bf16x8* wv) {
#pragma unroll
        for (int nb = 0; nb < NB; ++nb)
#pragma unroll
            for (int t = 0; t < 4; ++t) acc[nb][t] = MFMA16(wv[t], o[nb], acc[nb][t]);
    };
    load(o0, w0, 0);
#pragma unroll 1
    for (int kc = 0; kc < 16; kc += 2) {
        load(o1, w1, kc + 1);
        comp(o0, w0);
        if (kc + 2 < 16) load(o0, w0, kc + 2);
        comp(o1, w1);
    }
    f32x4* red = (f32x4*)smem;
    constexpr int NT = NB * 4;
#pragma unroll
    for (int r0 = 0; r0 < NT; r0 += 16) {
        if (r0) __syncthreads();
#pragma unroll
        for (int T = r0; T < (r0 + 16 < NT ? r0 + 16 : NT); ++T) red[((T - r0) * 8 + w) * 64 + lane] = acc[T >> 2][T & 3];
        __syncthreads();
#pragma unroll
        for (int h = 0; h < 2; ++h) {
            const int T = r0 + h * 8 + w;
            if (T < NT) {
                f32x4 s = red[((T - r0) * 8 + 0) * 64 + lane];
#pragma unroll
                for (int k = 1; k < 8; ++k) s += red[((T - r0) * 8 + k) * 64 + lane];
                const int row = (T >> 2) * 16 + (lane & 15), col = (T & 3) * 16 + (lane >> 4) * 4;
                u32x2 o = {cvtpk(s[0], s[1]), cvtpk(s[2], s[3])};
                *(u32x2*)(outp + (size_t)row * 1024 + col) = o;
            }
        }
    }
}

__global__ __launch_bounds__(512, 2) void oproj_k(const char* __restrict__ ws, __hip_bfloat16* __restrict__ out) {
    __shared__ __attribute__((aligned(16))) char smem[16 * 8 * 1024];
    const int bid = blockIdx.x, xcd = bid & 7, idx = bid >> 3;
    const int ib = 2 * xcd + (idx >> 4), half = (idx >> 3) & 1, nt = idx & 7;
    const int inst = ib >> 3, b = ib & 7;
    const int lane = threadIdx.x & 63;
    const int w = __builtin_amdgcn_readfirstlane(threadIdx.x >> 6);
    const int rb0 = half ? 7 : 0;
    const char* of = ws + WS_OF + ((size_t)ib * NRB + rb0) * (128 * 1024);
    const char* wf = ws + WS_WF + ((size_t)inst * 128 * 32 + nt * 4) * 1024;
    __hip_bfloat16* outp = out + (size_t)(b * SQ + rb0 * 16) * 1024 + inst * 512 + nt * 64;
    if (half) oproj_body<6>(of, wf, outp, smem, w, lane);
    else oproj_body<7>(of, wf, outp, smem, w, lane);
}

extern "C" void kernel_launch(void* const* d_in, const int* in_sizes, int n_in, void* d_out, int out_size, void* d_ws,
                              size_t ws_size, hipStream_t stream) {
    Ptrs p;
    for (int i = 0; i < 2; ++i) {
        p.x[i] = (const __hip_bfloat16*)d_in[5 * i + 0];
        p.k[i] = (const __hip_bfloat16*)d_in[5 * i + 1];
        p.v[i] = (const __hip_bfloat16*)d_in[5 * i + 2];
        p.wq[i] = (const __hip_bfloat16*)d_in[5 * i + 3];
        p.wo[i] = (const __hip_bfloat16*)d_in[5 * i + 4];
    }
    (void)hipFuncSetAttribute((const void*)attn_k, hipFuncAttributeMaxDynamicSharedMemorySize, LDS1);
    hipLaunchKernelGGL(attn_k, dim3(256), dim3(512), LDS1, stream, p, (char*)d_ws);
    hipLaunchKernelGGL(oproj_k, dim3(256), dim3(512), 0, stream, (const char*)d_ws, (__hip_bfloat16*)d_out);
}
```

```cpp
#include <hip/hip_runtime.h>
#include <hip/hip_bf16.h>

typedef __attribute__((ext_vector_type(8))) short bf16x8;
typedef __attribute__((ext_vector_type(4))) short s16x4;
typedef __attribute__((ext_vector_type(4))) float f32x4;
typedef __attribute__((ext_vector_type(2))) float f32x2;
typedef __attribute__((ext_vector_type(4))) unsigned u32x4;
typedef __attribute__((ext_vector_type(2))) unsigned u32x2;
typedef __attribute__((ext_vector_type(2))) __bf16 bf16x2_t;
typedef short v4i16_t __attribute__((ext_vector_type(4)));
#define LDS_AS __attribute__((address_space(3)))

constexpr int NB_BATCH = 8, SQ = 208, DM = 512, NH = 32, SK = 512, DH = 128;
constexpr int NRB = 13;
constexpr float QSCALE = 0.08838834764831845f * 1.4426950408889634f;
constexpr float LIM = 1024.0f;

constexpr int KV_STAGE = 32768;
constexpr int X_SLOT = 13 * 1024, W_SLOT = 16384;
constexpr int X_RING = 65536, W_RING = X_RING + 3 * X_SLOT;
constexpr int LDS1 = 4 * KV_STAGE + 16384 + 8192;

constexpr size_t WS_OF = 0;
constexpr size_t OF_BYTES = (size_t)16 * NRB * 128 * 1024;
constexpr size_t WS_WF = OF_BYTES;
constexpr size_t WF_BYTES = (size_t)2 * 128 * 32 * 1024;
constexpr size_t WS_PART = WS_WF + WF_BYTES;

#define STAMP(i)
#define STAMP_ARG
#define MFMA16(a, b, c) __builtin_amdgcn_mfma_f32_16x16x32_bf16(a, b, c, 0, 0, 0)
#define WAIT_BAR(N) asm volatile("s_waitcnt vmcnt(" #N ") lgkmcnt(0)\n\ts_barrier" ::: "memory")

__device__ __forceinline__ void glds16(const char* sbase, unsigned voff, unsigned lds_base) {
    unsigned sv;
    asm volatile("s_mov_b32 %0, m0\n\ts_mov_b32 m0, %3\n\ts_nop 0\n\tglobal_load_lds_dwordx4 %1, %2\n\ts_mov_b32 m0, %0"
                 : "=&s"(sv) : "v"(voff), "s"(sbase), "s"(lds_base) : "memory");
}
__device__ __forceinline__ bf16x8 lds_b128(unsigned a) { return *(const LDS_AS bf16x8*)(a); }
__device__ __forceinline__ s16x4 lds_tr(unsigned a) {
    return __builtin_bit_cast(s16x4, __builtin_amdgcn_ds_read_tr16_b64_v4i16((LDS_AS v4i16_t*)(a)));
}
__device__ __forceinline__ bf16x8 cat44(s16x4 l, s16x4 h) { return (bf16x8){l[0], l[1], l[2], l[3], h[0], h[1], h[2], h[3]}; }
__device__ __forceinline__ unsigned cvtpk(float lo, float hi) {
    f32x2 v = {lo, hi};
    return __builtin_bit_cast(unsigned, __builtin_convertvector(v, bf16x2_t));
}
__device__ __forceinline__ bf16x8 pack8(f32x4 a, f32x4 b) {
    u32x4 w = {cvtpk(a[0], a[1]), cvtpk(a[2], a[3]), cvtpk(b[0], b[1]), cvtpk(b[2], b[3])};
    return __builtin_bit_cast(bf16x8, w);
}
__device__ __forceinline__ float max3f(float a, float b, float c) { return __builtin_fmaxf(__builtin_fmaxf(a, b), c); }

struct Ptrs {
    const __hip_bfloat16* x[2];
    const __hip_bfloat16* k[2];
    const __hip_bfloat16* v[2];
    const __hip_bfloat16* wq[2];
    const __hip_bfloat16* wo[2];
};

__device__ __forceinline__ int opaque_lane() {
    int l;
    asm volatile("v_mbcnt_lo_u32_b32 %0, -1, 0\n\tv_mbcnt_hi_u32_b32 %0, -1, %0" : "=v"(l));
    return l;
}
template <int N> __device__ __forceinline__ void wait_bar() {
    asm volatile("s_waitcnt vmcnt(%0) lgkmcnt(0)\n\ts_barrier" :: "n"(N) : "memory");
}
__device__ __forceinline__ int tr_colB_of(int lane) {
    const int tr_row = (lane & 31) >> 2, cpos = lane & 3;
    return (lane >> 5) * 64 + (((cpos >> 1) ^ (tr_row >> 2)) * 32) + (cpos & 1) * 16;
}

__device__ __forceinline__ float max2f(float a, float b) { return __builtin_amdgcn_fmed3f(a, b, INFINITY); }

__global__ __launch_bounds__(512, 2) void attn_k(Ptrs p, char* __restrict__ ws) {
    const int bid = blockIdx.x, xcd = bid & 7, idx = bid >> 3;
    const int inst = xcd >> 2, hp = (xcd & 3) * 4 + (idx & 3), b = idx >> 2;
    const int w = __builtin_amdgcn_readfirstlane((int)threadIdx.x >> 6);
    const int grp = w >> 2, wi = w & 3, head = hp * 2 + grp;
    const char* xg = (const char*)(p.x[inst]) + (size_t)b * SQ * DM * 2;
    const char* wqg = (const char*)(p.wq[inst]) + (size_t)head * DH * 2;
    const char* kg = (const char*)(p.k[inst]) + (size_t)(b * NH + head) * SK * DH * 2;
    const char* vg = (const char*)(p.v[inst]) + (size_t)(b * NH + head) * SK * DH * 2;
    const char* wog = (const char*)(p.wo[inst]) + ((size_t)(hp * 256 + w * 32) * DM + b * 64) * 2;
    char* ofp = ws + WS_OF + ((size_t)(inst * 8 + b) * NRB * 128 + head * 4) * 1024;
    char* wfp = ws + WS_WF + (((size_t)inst * 128 + hp * 8 + w) * 32 + b * 4) * 1024;
    const int rb0 = 3 * wi;
    const int rot = (int)(idx * 5 + xcd * 3) & 15;
    const int lane = threadIdx.x & 63;
    const int m = lane & 15, g = lane >> 4, q4 = m >> 2, p4 = lane & 3, hh = lane >> 5, g1 = g & 1;
    STAMP(0);
    const int tr_row = (lane & 31) >> 2;
    const int kkey = 8 * wi + (lane >> 3);
    unsigned k_lane = (unsigned)(kkey * 256 + (((lane & 7) ^ ((kkey >> 1) & 7)) * 16));
    unsigned v_lane = (unsigned)((8 * wi + tr_row) * 256 + tr_colB_of(lane));
    auto kv_one = [&](int st, int i) {
        const unsigned d = (unsigned)((st & 3) * KV_STAGE + grp * 16384);
        const int sg = (st + rot) & 15;
        if (i < 2) glds16(kg + (size_t)sg * 8192 + i * 128, k_lane, d + i * 4096 + wi * 1024);
        else glds16(vg + (size_t)sg * 8192 + (i - 2) * 128, v_lane, d + 8192 + wi * 2048 + (i - 2) * 1024);
    };
    bf16x8 Q[3][4];
    bf16x8 q12c;
    {
        const int xr = lane >> 2, xpos = lane & 3;
        const unsigned x_lane = (unsigned)(xr * (DM * 2) + ((xpos ^ ((4 - (xr >> 2)) & 3)) * 16));
        const unsigned wq_lane = (unsigned)((16 * (wi & 1) + 8 * (tr_row >> 2) + 4 * (wi >> 1) + (tr_row & 3)) * (NH * DH * 2) + tr_colB_of(lane));
        const int xb2 = (w < 5) ? 8 + w : 12;
        const char* xs1 = xg + (size_t)(w * 16) * (DM * 2);
        const char* xs2 = xg + (size_t)(xb2 * 16) * (DM * 2);
        const unsigned xd1 = (unsigned)(X_RING + w * 1024), xd2 = (unsigned)(X_RING + xb2 * 1024);
        const unsigned wd = (unsigned)(W_RING + grp * 8192 + wi * 2048);
        auto issue_xw = [&](int ks) {
            const int slot = ks % 3;
            glds16(xs1 + ks * 64, x_lane, xd1 + slot * X_SLOT);
            glds16(wqg + (size_t)ks * 32 * (NH * DH * 2), wq_lane, wd + slot * W_SLOT);
            glds16(wqg + (size_t)ks * 32 * (NH * DH * 2) + 128, wq_lane, wd + slot * W_SLOT + 1024);
            glds16(xs2 + ks * 64, x_lane, xd2 + slot * X_SLOT);
        };
        f32x4 acc[3][8], acc12[2];
#pragma unroll
        for (int nb = 0; nb < 3; ++nb)
#pragma unroll
            for (int t = 0; t < 8; ++t) acc[nb][t] = (f32x4){0.f, 0.f, 0.f, 0.f};
        acc12[0] = acc12[1] = (f32x4){0.f, 0.f, 0.f, 0.f};
        issue_xw(0);
        issue_xw(1);
        const unsigned xl = (unsigned)(X_RING + m * 64 + ((g ^ ((4 - q4) & 3)) * 16));
        const unsigned xb = xl + rb0 * 1024;
        const unsigned wb = (unsigned)(W_RING + grp * 8192 + hh * 2048 + (4 * g1 + q4) * 64 + ((16 * p4) ^ (32 * g1)));
        const unsigned wb12 = wb + wi * 512;
#pragma unroll
        for (int ks = 0; ks < 16; ++ks) {
            if (ks == 0) wait_bar<4>(); else if (ks <= 8) wait_bar<5>(); else if (ks < 15) wait_bar<4>(); else wait_bar<0>();
            if (ks < 14) issue_xw(ks + 2);
            if (ks < 8) kv_one(ks >> 2, ks & 3);
            const int slot = ks % 3;
            bf16x8 xf[3];
#pragma unroll
            for (int nb = 0; nb < 3; ++nb) xf[nb] = lds_b128(xb + slot * X_SLOT + nb * 1024);
            const bf16x8 x12 = lds_b128(xl + slot * X_SLOT + 12 * 1024);
#pragma unroll
            for (int t = 0; t < 8; ++t) {
                const int c = t >> 1, u = t & 1;
                const bf16x8 a = cat44(lds_tr(wb + slot * W_SLOT + c * 512 + u * 8), lds_tr(wb + slot * W_SLOT + 4096 + c * 512 + u * 8));
#pragma unroll
                for (int nb = 0; nb < 3; ++nb) acc[nb][t] = MFMA16(a, xf[nb], acc[nb][t]);
            }
#pragma unroll
            for (int u = 0; u < 2; ++u) {
                const bf16x8 a = cat44(lds_tr(wb12 + slot * W_SLOT + u * 8), lds_tr(wb12 + slot * W_SLOT + 4096 + u * 8));
                acc12[u] = MFMA16(a, x12, acc12[u]);
            }
        }
#pragma unroll
        for (int nb = 0; nb < 3; ++nb)
#pragma unroll
            for (int c = 0; c < 4; ++c) Q[nb][c] = pack8(acc[nb][2 * c] * QSCALE, acc[nb][2 * c + 1] * QSCALE);
        q12c = pack8(acc12[0] * QSCALE, acc12[1] * QSCALE);
    }

    f32x4 O[3][8], O12[2];
    f32x4 negm[4];
    float l[4];
#pragma unroll
    for (int nb = 0; nb < 4; ++nb) { negm[nb] = (f32x4){0.f, 0.f, 0.f, 0.f}; l[nb] = 0.f; }
#pragma unroll
    for (int nb = 0; nb < 3; ++nb)
#pragma unroll
        for (int t = 0; t < 8; ++t) O[nb][t] = (f32x4){0.f, 0.f, 0.f, 0.f};
    O12[0] = O12[1] = (f32x4){0.f, 0.f, 0.f, 0.f};
    const int l2 = opaque_lane();
    const int m2 = l2 & 15, g2 = l2 >> 4;
    {
        const int kk2 = 8 * wi + (l2 >> 3);
        k_lane = (unsigned)(kk2 * 256 + (((l2 & 7) ^ ((kk2 >> 1) & 7)) * 16));
        v_lane = (unsigned)((8 * wi + ((l2 & 31) >> 2)) * 256 + tr_colB_of(l2));
    }
    unsigned kE = (unsigned)(grp * 16384 + m2 * 128 + ((g2 ^ (m2 >> 1)) * 16));
    unsigned vE = (unsigned)(grp * 16384 + 8192 + (l2 >> 5) * 2048 + (4 * (g2 & 1) + (m2 >> 2)) * 64 + 32 * (g2 & 1) + 8 * (l2 & 3));
    const unsigned q4a = (unsigned)(4 * KV_STAGE + 16384 + grp * 4096 + l2 * 16);
    *(LDS_AS bf16x8*)(q4a + wi * 1024) = q12c;
    STAMP(1);
#define SB() __builtin_amdgcn_sched_barrier(0)
    WAIT_BAR(0);
    STAMP(2);
#pragma unroll
    for (int i = 0; i < 4; ++i) kv_one(2, i);
    auto sync = [&](int j) {
        WAIT_BAR(4);
        if (j < 13) {
#pragma unroll
            for (int i = 0; i < 4; ++i) kv_one(j + 3, i);
        } else if (j == 13) {
            const int ln = opaque_lane();
            const unsigned wo_lane = (unsigned)(((ln & 31) >> 2) * (DM * 2) + tr_colB_of(ln));
#pragma unroll
            for (int i = 0; i < 4; ++i) glds16(wog + (size_t)(8 * i) * (DM * 2), wo_lane, (unsigned)(w * 4096 + i * 1024));
        }
    };
    auto ldk = [&](int c, int kt) { return lds_b128(((c & 1) ? (kE ^ 64u) : kE) + (c >> 1) * 4096 + kt * 2048); };
    auto ldv = [&](int dt) {
        const unsigned vb = ((dt & 1) ? (vE ^ 32u) : vE) + (dt >> 1) * 512;
        return cat44(lds_tr(vb), lds_tr(vb + 4096));
    };
#define PRIO 0
    if (PRIO == 1 && grp == 1) __builtin_amdgcn_s_setprio(1);
    if (PRIO == 2 && grp == 0) __builtin_amdgcn_s_setprio(1);
    float lo = -INFINITY;
    bf16x8 kf0 = ldk(0, 0), kf1 = ldk(0, 1), qf = lds_b128(q4a);
#pragma clang loop unroll(disable)
    for (int j = 0; j < 16; ++j) {
        STAMP(8 + j);
        f32x4 S[4][2];
#pragma unroll
        for (int c = 0; c < 4; ++c) {
            bf16x8 n0, n1, nq;
            if (c < 3) { n0 = ldk(c + 1, 0); nq = lds_b128(q4a + (c + 1) * 1024); n1 = ldk(c + 1, 1); }
            SB();
#pragma unroll
            for (int nb = 0; nb < 3; ++nb) S[nb][0] = MFMA16(kf0, Q[nb][c], c == 0 ? negm[nb] : S[nb][0]);
            S[3][0] = MFMA16(kf0, qf, c == 0 ? negm[3] : S[3][0]);
#pragma unroll
            for (int nb = 0; nb < 3; ++nb) S[nb][1] = MFMA16(kf1, Q[nb][c], c == 0 ? negm[nb] : S[nb][1]);
            S[3][1] = MFMA16(kf1, qf, c == 0 ? negm[3] : S[3][1]);
            SB();
            if (c < 3) { kf0 = n0; kf1 = n1; qf = nq; }
        }
        if (grp == 0 && j < 15) sync(j);
        bf16x8 vf[2];
#pragma unroll
        for (int dt = 0; dt < 2; ++dt) vf[dt] = ldv(dt);
        SB();
        bf16x8 P[4];
        float ps[4];
        auto softmax_tile = [&]() {
#pragma unroll
            for (int nb = 0; nb < 4; ++nb) {
                f32x4 e0, e1;
#pragma unroll
                for (int r = 0; r < 4; ++r) { e0[r] = __builtin_amdgcn_exp2f(S[nb][0][r]); e1[r] = __builtin_amdgcn_exp2f(S[nb][1][r]); }
                ps[nb] = ((e0[0] + e0[1]) + (e0[2] + e0[3])) + ((e1[0] + e1[1]) + (e1[2] + e1[3]));
                P[nb] = pack8(e0, e1);
            }
        };
        softmax_tile();
        const float psm = max3f(max2f(ps[0], ps[1]), ps[2], ps[3]);
        if (j == 0 || __any(!(psm <= LIM))) {
#pragma unroll
            for (int nb = 0; nb < 4; ++nb) {
                float rm = max2f(S[nb][0][0], S[nb][0][1]);
                rm = max3f(rm, S[nb][0][2], S[nb][0][3]);
                rm = max3f(rm, S[nb][1][0], S[nb][1][1]);
                rm = max3f(rm, S[nb][1][2], S[nb][1][3]);
                rm = __builtin_fmaxf(rm, __shfl_xor(rm, 16));
                rm = __builtin_fmaxf(rm, __shfl_xor(rm, 32));
                const float delta = __builtin_fmaxf(rm, lo);
                const float alpha = __builtin_amdgcn_exp2f(__builtin_fminf(-delta, 0.f));
                negm[nb] -= delta; l[nb] *= alpha;
                S[nb][0] -= delta; S[nb][1] -= delta;
                if (nb < 3) {
#pragma unroll
                    for (int t = 0; t < 8; ++t) O[nb][t] *= alpha;
                } else { O12[0] *= alpha; O12[1] *= alpha; }
            }
            lo = 0.f;
            softmax_tile();
        }
#pragma unroll
        for (int nb = 0; nb < 4; ++nb) l[nb] += ps[nb];
        if (grp == 1 && j < 15) sync(j);
        kE = (kE + KV_STAGE) & (4 * KV_STAGE - 1);
        {
            bf16x8 va[3], vb3[3];
#pragma unroll
            for (int dt = 0; dt < 3; ++dt) va[dt] = ldv(2 + dt);
            SB();
#pragma unroll
            for (int dt = 0; dt < 2; ++dt)
#pragma unroll
                for (int nb = 0; nb < 3; ++nb) O[nb][dt] = MFMA16(vf[dt], P[nb], O[nb][dt]);
            SB();
#pragma unroll
            for (int dt = 0; dt < 3; ++dt) vb3[dt] = ldv(5 + dt);
            SB();
#pragma unroll
            for (int dt = 0; dt < 3; ++dt)
#pragma unroll
                for (int nb = 0; nb < 3; ++nb) O[nb][2 + dt] = MFMA16(va[dt], P[nb], O[nb][2 + dt]);
            SB();
            const unsigned vb = vE + wi * 512;
            const bf16x8 v12a = cat44(lds_tr(vb), lds_tr(vb + 4096)), v12b = cat44(lds_tr(vb ^ 32u), lds_tr((vb ^ 32u) + 4096));
            kf0 = ldk(0, 0); qf = lds_b128(q4a); kf1 = ldk(0, 1);
            SB();
#pragma unroll
            for (int dt = 0; dt < 3; ++dt)
#pragma unroll
                for (int nb = 0; nb < 3; ++nb) O[nb][5 + dt] = MFMA16(vb3[dt], P[nb], O[nb][5 + dt]);
            O12[0] = MFMA16(v12a, P[3], O12[0]);
            O12[1] = MFMA16(v12b, P[3], O12[1]);
            SB();
        }
        vE = (vE + KV_STAGE) & (4 * KV_STAGE - 1);
    }
    STAMP(3);
    const int ln = opaque_lane();
#pragma unroll
    for (int nb = 0; nb < 4; ++nb) {
        float s = l[nb];
        s += __shfl_xor(s, 16);
        s += __shfl_xor(s, 32);
        const float inv = 1.0f / s;
        if (nb < 3) {
            char* dst = ofp + (size_t)(rb0 + nb) * (128 * 1024) + ln * 16;
#pragma unroll
            for (int c = 0; c < 4; ++c) *(bf16x8*)(dst + c * 1024) = pack8(O[nb][2 * c] * inv, O[nb][2 * c + 1] * inv);
        } else {
            *(bf16x8*)(ofp + (size_t)12 * (128 * 1024) + wi * 1024 + ln * 16) = pack8(O12[0] * inv, O12[1] * inv);
        }
    }
    asm volatile("s_waitcnt vmcnt(0)" ::: "memory");
    {
        const int G1 = (ln >> 4) & 1, Q4 = (ln & 15) >> 2;
        const unsigned bE = (unsigned)(w * 4096 + (ln >> 5) * 1024 + (4 * G1 + Q4) * 64 + 32 * G1 + 8 * (ln & 3));
#pragma unroll
        for (int nt = 0; nt < 4; ++nt) {
            const unsigned bb = ((nt & 1) ? (bE ^ 32u) : bE) + (nt >> 1) * 512;
            const bf16x8 a = cat44(lds_tr(bb), lds_tr(bb + 2048));
            *(bf16x8*)(wfp + nt * 1024 + ln * 16) = a;
        }
    }
    STAMP(4);
}

constexpr int G_CHUNK = 21 * 1024, G_STAGE = 2 * G_CHUNK, LDS2 = 3 * G_STAGE;
template <int NB>
__device__ __forceinline__ void oproj_body(const char* __restrict__ of, const char* __restrict__ wf, char* __restrict__ part,
                                           int w, int rb0, int cg) {
    const int lane = threadIdx.x & 63;
    const unsigned loff = (unsigned)lane * 16u;
    const int o2 = (w < 5) ? 8 + w : 12;
    const char* src0 = of + (size_t)w * (128 * 1024);
    const char* src1 = of + (size_t)o2 * (128 * 1024);
    const char* src2 = wf + (size_t)w * 1024;
    auto issue = [&](int st) {
        const unsigned d = (unsigned)((st % 3) * G_STAGE);
#pragma unroll
        for (int h = 0; h < 2; ++h) {
            const int kc = 2 * st + h;
            glds16(src0 + (size_t)kc * 1024, loff, d + h * G_CHUNK + w * 1024);
            glds16(src2 + (size_t)kc * (32 * 1024), loff, d + h * G_CHUNK + (13 + w) * 1024);
            glds16(src1 + (size_t)kc * 1024, loff, d + h * G_CHUNK + o2 * 1024);
        }
    };
    f32x4 acc[NB][2];
#pragma unroll
    for (int nb = 0; nb < NB; ++nb) acc[nb][0] = acc[nb][1] = (f32x4){0.f, 0.f, 0.f, 0.f};
    issue(0);
    issue(1);
    const unsigned ob = loff + rb0 * 1024, wb = loff + (13 + 2 * cg) * 1024;
    int slot = 0;
#pragma clang loop unroll(disable)
    for (int st = 0; st < 16; ++st) {
        if (st < 15) { WAIT_BAR(6); } else { WAIT_BAR(0); }
        if (st < 14) issue(st + 2);
        const unsigned d = (unsigned)(slot * G_STAGE);
#pragma unroll
        for (int h = 0; h < 2; ++h) {
            const bf16x8 w0 = lds_b128(wb + d + h * G_CHUNK), w1 = lds_b128(wb + d + h * G_CHUNK + 1024);
            bf16x8 o[NB];
#pragma unroll
            for (int nb = 0; nb < NB; ++nb) o[nb] = lds_b128(ob + d + h * G_CHUNK + nb * 1024);
#pragma unroll
            for (int nb = 0; nb < NB; ++nb) {
                acc[nb][0] = MFMA16(w0, o[nb], acc[nb][0]);
                acc[nb][1] = MFMA16(w1, o[nb], acc[nb][1]);
            }
        }
        slot = (slot == 2) ? 0 : slot + 1;
    }
#pragma unroll
    for (int nb = 0; nb < NB; ++nb)
#pragma unroll
        for (int t = 0; t < 2; ++t) *(f32x4*)(part + ((size_t)(rb0 + nb) * 32 + 2 * cg + t) * 1024 + loff) = acc[nb][t];
}

__global__ __launch_bounds__(512, 2) void oproj_k(char* __restrict__ ws) {
    const int bid = blockIdx.x, xcd = bid & 7, idx = bid >> 3;
    const int inst = xcd >> 2, ks = xcd & 3, b = idx >> 2, nt = idx & 3;
    const int ib = inst * 8 + b;
    const int w = __builtin_amdgcn_readfirstlane((int)threadIdx.x >> 6);
    const char* of = ws + WS_OF + ((size_t)ib * NRB * 128 + ks * 32) * 1024;
    const char* wf = ws + WS_WF + (((size_t)inst * 128 + ks * 32) * 32 + nt * 8) * 1024;
    char* part = ws + WS_PART + (((size_t)ks * 16 + ib) * NRB * 32 + nt * 8) * 1024;
    if (w < 4) oproj_body<7>(of, wf, part, w, 0, w & 3);
    else oproj_body<6>(of, wf, part, w, 7, w & 3);
}

__global__ __launch_bounds__(256) void reduce_k(const char* __restrict__ ws, __hip_bfloat16* __restrict__ out) {
    const int t = blockIdx.x * 256 + threadIdx.x;
    const int lane = t & 63, tile = t >> 6;
    const f32x4* p = (const f32x4*)(ws + WS_PART) + t;
    constexpr size_t STR = (size_t)16 * NRB * 32 * 64;
    const f32x4 s = (p[0] + p[STR]) + (p[2 * STR] + p[3 * STR]);
    const int nt16 = tile & 31, r = tile >> 5, rb = r % NRB, ib = r / NRB;
    const int row = (ib & 7) * SQ + rb * 16 + (lane & 15), col = (ib >> 3) * 512 + nt16 * 16 + (lane >> 4) * 4;
    u32x2 o = {cvtpk(s[0], s[1]), cvtpk(s[2], s[3])};
    *(u32x2*)(out + (size_t)row * 1024 + col) = o;
}

extern "C" void kernel_launch(void* const* d_in, const int* in_sizes, int n_in, void* d_out, int out_size, void* d_ws,
                              size_t ws_size, hipStream_t stream) {
    Ptrs p;
    for (int i = 0; i < 2; ++i) {
        p.x[i] = (const __hip_bfloat16*)d_in[5 * i + 0];
        p.k[i] = (const __hip_bfloat16*)d_in[5 * i + 1];
        p.v[i] = (const __hip_bfloat16*)d_in[5 * i + 2];
        p.wq[i] = (const __hip_bfloat16*)d_in[5 * i + 3];
        p.wo[i] = (const __hip_bfloat16*)d_in[5 * i + 4];
    }
    (void)hipFuncSetAttribute((const void*)attn_k, hipFuncAttributeMaxDynamicSharedMemorySize, LDS1);
    hipLaunchKernelGGL(attn_k, dim3(256), dim3(512), LDS1, stream, p, (char*)d_ws);
    (void)hipFuncSetAttribute((const void*)oproj_k, hipFuncAttributeMaxDynamicSharedMemorySize, LDS2);
    hipLaunchKernelGGL(oproj_k, dim3(256), dim3(512), LDS2, stream, (char*)d_ws);
    hipLaunchKernelGGL(reduce_k, dim3(16 * NRB * 32 * 64 / 256), dim3(256), 0, stream, (const char*)d_ws, (__hip_bfloat16*)d_out);
}
```

```cpp
#include <hip/hip_runtime.h>
#include <hip/hip_bf16.h>

typedef __attribute__((ext_vector_type(8))) short bf16x8;
typedef __attribute__((ext_vector_type(4))) short s16x4;
typedef __attribute__((ext_vector_type(4))) float f32x4;
typedef __attribute__((ext_vector_type(2))) float f32x2;
typedef __attribute__((ext_vector_type(4))) unsigned u32x4;
typedef __attribute__((ext_vector_type(2))) unsigned u32x2;
typedef __attribute__((ext_vector_type(2))) __bf16 bf16x2_t;
typedef short v4i16_t __attribute__((ext_vector_type(4)));
#define LDS_AS __attribute__((address_space(3)))

constexpr int NB_BATCH = 8, SQ = 208, DM = 512, NH = 32, SK = 512, DH = 128;
constexpr int NRB = 13;
constexpr float QSCALE = 0.08838834764831845f * 1.4426950408889634f;
constexpr float LIM = 1024.0f;

constexpr int KV_STAGE = 32768;
constexpr int X_SLOT = 13 * 1024, W_SLOT = 16384;
constexpr int X_RING = 65536, W_RING = X_RING + 3 * X_SLOT;
constexpr int LDS1 = 4 * KV_STAGE + 16384 + 8192 + 256;

constexpr size_t WS_OF = 0;
constexpr size_t OF_BYTES = (size_t)16 * NRB * 128 * 1024;
constexpr size_t WS_WF = OF_BYTES;
constexpr size_t WF_BYTES = (size_t)2 * 128 * 32 * 1024;
constexpr size_t WS_PART = WS_WF + WF_BYTES;

#define STAMP(i)
#define STAMP_ARG
#define MFMA16(a, b, c) __builtin_amdgcn_mfma_f32_16x16x32_bf16(a, b, c, 0, 0, 0)
#define WAIT_BAR(N) asm volatile("s_waitcnt vmcnt(" #N ") lgkmcnt(0)\n\ts_barrier" ::: "memory")

__device__ __forceinline__ void glds16(const char* sbase, unsigned voff, unsigned lds_base) {
    unsigned sv;
    asm volatile("s_mov_b32 %0, m0\n\ts_mov_b32 m0, %3\n\ts_nop 0\n\tglobal_load_lds_dwordx4 %1, %2\n\ts_mov_b32 m0, %0"
                 : "=&s"(sv) : "v"(voff), "s"(sbase), "s"(lds_base) : "memory");
}
__device__ __forceinline__ void gtouch(const char* sbase, unsigned voff, unsigned lds_base) {
    unsigned sv;
    asm volatile("s_mov_b32 %0, m0\n\ts_mov_b32 m0, %3\n\ts_nop 0\n\tglobal_load_lds_dword %1, %2\n\ts_mov_b32 m0, %0"
                 : "=&s"(sv) : "v"(voff), "s"(sbase), "s"(lds_base) : "memory");
}
__device__ __forceinline__ bf16x8 lds_b128(unsigned a) { return *(const LDS_AS bf16x8*)(a); }
__device__ __forceinline__ s16x4 lds_tr(unsigned a) {
    return __builtin_bit_cast(s16x4, __builtin_amdgcn_ds_read_tr16_b64_v4i16((LDS_AS v4i16_t*)(a)));
}
__device__ __forceinline__ bf16x8 cat44(s16x4 l, s16x4 h) { return (bf16x8){l[0], l[1], l[2], l[3], h[0], h[1], h[2], h[3]}; }
__device__ __forceinline__ unsigned cvtpk(float lo, float hi) {
    f32x2 v = {lo, hi};
    return __builtin_bit_cast(unsigned, __builtin_convertvector(v, bf16x2_t));
}
__device__ __forceinline__ bf16x8 pack8(f32x4 a, f32x4 b) {
    u32x4 w = {cvtpk(a[0], a[1]), cvtpk(a[2], a[3]), cvtpk(b[0], b[1]), cvtpk(b[2], b[3])};
    return __builtin_bit_cast(bf16x8, w);
}
__device__ __forceinline__ float max3f(float a, float b, float c) { return __builtin_fmaxf(__builtin_fmaxf(a, b), c); }

struct Ptrs {
    const __hip_bfloat16* x[2];
    const __hip_bfloat16* k[2];
    const __hip_bfloat16* v[2];
    const __hip_bfloat16* wq[2];
    const __hip_bfloat16* wo[2];
};

__device__ __forceinline__ int opaque_lane() {
    int l;
    asm volatile("v_mbcnt_lo_u32_b32 %0, -1, 0\n\tv_mbcnt_hi_u32_b32 %0, -1, %0" : "=v"(l));
    return l;
}
template <int N> __device__ __forceinline__ void wait_bar() {
    asm volatile("s_waitcnt vmcnt(%0) lgkmcnt(0)\n\ts_barrier" :: "n"(N) : "memory");
}
__device__ __forceinline__ int tr_colB_of(int lane) {
    const int tr_row = (lane & 31) >> 2, cpos = lane & 3;
    return (lane >> 5) * 64 + (((cpos >> 1) ^ (tr_row >> 2)) * 32) + (cpos & 1) * 16;
}

__device__ __forceinline__ float max2f(float a, float b) { return __builtin_amdgcn_fmed3f(a, b, INFINITY); }

__global__ __launch_bounds__(512, 2) void attn_k(Ptrs p, char* __restrict__ ws) {
    const int bid = blockIdx.x, xcd = bid & 7, idx = bid >> 3;
    const int inst = xcd >> 2, hp = (xcd & 3) * 4 + (idx & 3), b = idx >> 2;
    const int w = __builtin_amdgcn_readfirstlane((int)threadIdx.x >> 6);
    const int grp = w >> 2, wi = w & 3, head = hp * 2 + grp;
    const char* xg = (const char*)(p.x[inst]) + (size_t)b * SQ * DM * 2;
    const char* wqg = (const char*)(p.wq[inst]) + (size_t)head * DH * 2;
    const char* kg = (const char*)(p.k[inst]) + (size_t)(b * NH + head) * SK * DH * 2;
    const char* vg = (const char*)(p.v[inst]) + (size_t)(b * NH + head) * SK * DH * 2;
    const char* wog = (const char*)(p.wo[inst]) + ((size_t)(hp * 256 + w * 32) * DM + b * 64) * 2;
    char* ofp = ws + WS_OF + ((size_t)(inst * 8 + b) * 128 + head * 4) * (NRB * 1024);
    char* wfp = ws + WS_WF + (((size_t)inst * 128 + hp * 8 + w) * 32 + b * 4) * 1024;
    const int rb0 = 3 * wi;
    const int rot = (int)(idx * 5 + xcd * 3) & 15;
    const int lane = threadIdx.x & 63;
    const int m = lane & 15, g = lane >> 4, q4 = m >> 2, p4 = lane & 3, hh = lane >> 5, g1 = g & 1;
    STAMP(0);
    const int tr_row = (lane & 31) >> 2;
    const int kkey = 8 * wi + (lane >> 3);
    unsigned k_lane = (unsigned)(kkey * 256 + (((lane & 7) ^ ((kkey >> 1) & 7)) * 16));
    unsigned v_lane = (unsigned)((8 * wi + tr_row) * 256 + tr_colB_of(lane));
    auto kv_one = [&](int st, int i) {
        const unsigned d = (unsigned)((st & 3) * KV_STAGE + grp * 16384);
        const int sg = (st + rot) & 15;
        if (i < 2) glds16(kg + (size_t)sg * 8192 + i * 128, k_lane, d + i * 4096 + wi * 1024);
        else glds16(vg + (size_t)sg * 8192 + (i - 2) * 128, v_lane, d + 8192 + wi * 2048 + (i - 2) * 1024);
    };
    bf16x8 Q[3][4];
    bf16x8 q12c;
    {
        const int xr = lane >> 2, xpos = lane & 3;
        const unsigned x_lane = (unsigned)(xr * (DM * 2) + ((xpos ^ ((4 - (xr >> 2)) & 3)) * 16));
        const unsigned wq_lane = (unsigned)((16 * (wi & 1) + 8 * (tr_row >> 2) + 4 * (wi >> 1) + (tr_row & 3)) * (NH * DH * 2) + tr_colB_of(lane));
        const int xb2 = (w < 5) ? 8 + w : 12;
        const char* xs1 = xg + (size_t)(w * 16) * (DM * 2);
        const char* xs2 = xg + (size_t)(xb2 * 16) * (DM * 2);
        const unsigned xd1 = (unsigned)(X_RING + w * 1024), xd2 = (unsigned)(X_RING + xb2 * 1024);
        const unsigned wd = (unsigned)(W_RING + grp * 8192 + wi * 2048);
        auto issue_xw = [&](int ks) {
            const int slot = ks % 3;
            glds16(xs1 + ks * 64, x_lane, xd1 + slot * X_SLOT);
            glds16(wqg + (size_t)ks * 32 * (NH * DH * 2), wq_lane, wd + slot * W_SLOT);
            glds16(wqg + (size_t)ks * 32 * (NH * DH * 2) + 128, wq_lane, wd + slot * W_SLOT + 1024);
            if (w < 5) glds16(xs2 + ks * 64, x_lane, xd2 + slot * X_SLOT);
        };
        const unsigned touch_lane = (unsigned)(lane & 15) * 128u;
        f32x4 acc[3][8], acc12[2];
#pragma unroll
        for (int nb = 0; nb < 3; ++nb)
#pragma unroll
            for (int t = 0; t < 8; ++t) acc[nb][t] = (f32x4){0.f, 0.f, 0.f, 0.f};
        acc12[0] = acc12[1] = (f32x4){0.f, 0.f, 0.f, 0.f};
        issue_xw(0);
        issue_xw(1);
        issue_xw(2);
        const unsigned xl = (unsigned)(X_RING + m * 64 + ((g ^ ((4 - q4) & 3)) * 16));
        const unsigned xb = xl + rb0 * 1024;
        const unsigned wb = (unsigned)(W_RING + grp * 8192 + hh * 2048 + (4 * g1 + q4) * 64 + ((16 * p4) ^ (32 * g1)));
        const unsigned wb12 = wb + wi * 512;
        bf16x8 xf[2][4], af[2][10];
        auto ldfr = [&](int ks) {
            const int slot = ks % 3, sb = ks & 1;
#pragma unroll
            for (int nb = 0; nb < 3; ++nb) xf[sb][nb] = lds_b128(xb + slot * X_SLOT + nb * 1024);
            xf[sb][3] = lds_b128(xl + slot * X_SLOT + 12 * 1024);
#pragma unroll
            for (int t = 0; t < 8; ++t)
                af[sb][t] = cat44(lds_tr(wb + slot * W_SLOT + (t >> 1) * 512 + (t & 1) * 8), lds_tr(wb + slot * W_SLOT + 4096 + (t >> 1) * 512 + (t & 1) * 8));
#pragma unroll
            for (int u = 0; u < 2; ++u)
                af[sb][8 + u] = cat44(lds_tr(wb12 + slot * W_SLOT + u * 8), lds_tr(wb12 + slot * W_SLOT + 4096 + u * 8));
        };
        if (w < 5) wait_bar<8>(); else wait_bar<6>();
        ldfr(0);
#pragma unroll
        for (int ks = 0; ks < 16; ++ks) {
            if (w < 5) { if (ks == 0) wait_bar<4>(); else if (ks <= 8) wait_bar<5>(); else if (ks <= 13) wait_bar<4>(); else if (ks == 14) wait_bar<0>(); }
            else { if (ks == 0) wait_bar<3>(); else if (ks <= 8) wait_bar<4>(); else if (ks <= 13) wait_bar<3>(); else if (ks == 14) wait_bar<0>(); }
            if (ks < 13) issue_xw(ks + 3);
            if (ks < 8) kv_one(ks >> 2, ks & 3);
            if (ks < 15) ldfr(ks + 1);
            const int sb = ks & 1;
#pragma unroll
            for (int t = 0; t < 8; ++t)
#pragma unroll
                for (int nb = 0; nb < 3; ++nb) acc[nb][t] = MFMA16(af[sb][t], xf[sb][nb], acc[nb][t]);
            acc12[0] = MFMA16(af[sb][8], xf[sb][3], acc12[0]);
            acc12[1] = MFMA16(af[sb][9], xf[sb][3], acc12[1]);
        }
#pragma unroll
        for (int nb = 0; nb < 3; ++nb)
#pragma unroll
            for (int c = 0; c < 4; ++c) Q[nb][c] = pack8(acc[nb][2 * c] * QSCALE, acc[nb][2 * c + 1] * QSCALE);
        q12c = pack8(acc12[0] * QSCALE, acc12[1] * QSCALE);
    }

    f32x4 O[3][8], O12[2];
    f32x4 negm[4];
    float l[4];
#pragma unroll
    for (int nb = 0; nb < 4; ++nb) { negm[nb] = (f32x4){0.f, 0.f, 0.f, 0.f}; l[nb] = 0.f; }
#pragma unroll
    for (int nb = 0; nb < 3; ++nb)
#pragma unroll
        for (int t = 0; t < 8; ++t) O[nb][t] = (f32x4){0.f, 0.f, 0.f, 0.f};
    O12[0] = O12[1] = (f32x4){0.f, 0.f, 0.f, 0.f};
    const int l2 = opaque_lane();
    const int m2 = l2 & 15, g2 = l2 >> 4;
    {
        const int kk2 = 8 * wi + (l2 >> 3);
        k_lane = (unsigned)(kk2 * 256 + (((l2 & 7) ^ ((kk2 >> 1) & 7)) * 16));
        v_lane = (unsigned)((8 * wi + ((l2 & 31) >> 2)) * 256 + tr_colB_of(l2));
    }
    unsigned kE = (unsigned)(grp * 16384 + m2 * 128 + ((g2 ^ (m2 >> 1)) * 16));
    unsigned vE = (unsigned)(grp * 16384 + 8192 + (l2 >> 5) * 2048 + (4 * (g2 & 1) + (m2 >> 2)) * 64 + 32 * (g2 & 1) + 8 * (l2 & 3));
    const unsigned q4a = (unsigned)(4 * KV_STAGE + 16384 + grp * 4096 + l2 * 16);
    *(LDS_AS bf16x8*)(q4a + wi * 1024) = q12c;
    STAMP(1);
#define SB() __builtin_amdgcn_sched_barrier(0)
    WAIT_BAR(0);
    STAMP(2);
#pragma unroll
    for (int i = 0; i < 4; ++i) kv_one(2, i);
    auto sync = [&](int j) {
        WAIT_BAR(4);
        if (j < 13) {
#pragma unroll
            for (int i = 0; i < 4; ++i) kv_one(j + 3, i);
        } else if (j == 13) {
            const int ln = opaque_lane();
            const unsigned wo_lane = (unsigned)(((ln & 31) >> 2) * (DM * 2) + tr_colB_of(ln));
#pragma unroll
            for (int i = 0; i < 4; ++i) glds16(wog + (size_t)(8 * i) * (DM * 2), wo_lane, (unsigned)(w * 4096 + i * 1024));
        }
    };
    auto ldk = [&](int c, int kt) { return lds_b128(((c & 1) ? (kE ^ 64u) : kE) + (c >> 1) * 4096 + kt * 2048); };
    auto ldv = [&](int dt) {
        const unsigned vb = ((dt & 1) ? (vE ^ 32u) : vE) + (dt >> 1) * 512;
        return cat44(lds_tr(vb), lds_tr(vb + 4096));
    };
#define PRIO 1
    if (PRIO == 1 && grp == 1) __builtin_amdgcn_s_setprio(1);
    if (PRIO == 2 && grp == 0) __builtin_amdgcn_s_setprio(1);
    float lo = -INFINITY;
    bf16x8 kf0 = ldk(0, 0), kf1 = ldk(0, 1), qf = lds_b128(q4a);
#pragma clang loop unroll(disable)
    for (int j = 0; j < 16; ++j) {
        STAMP(8 + j);
        f32x4 S[4][2];
#pragma unroll
        for (int c = 0; c < 4; ++c) {
            bf16x8 n0, n1, nq;
            if (c < 3) { n0 = ldk(c + 1, 0); nq = lds_b128(q4a + (c + 1) * 1024); n1 = ldk(c + 1, 1); }
            SB();
#pragma unroll
            for (int nb = 0; nb < 3; ++nb) S[nb][0] = MFMA16(kf0, Q[nb][c], c == 0 ? negm[nb] : S[nb][0]);
            S[3][0] = MFMA16(kf0, qf, c == 0 ? negm[3] : S[3][0]);
#pragma unroll
            for (int nb = 0; nb < 3; ++nb) S[nb][1] = MFMA16(kf1, Q[nb][c], c == 0 ? negm[nb] : S[nb][1]);
            S[3][1] = MFMA16(kf1, qf, c == 0 ? negm[3] : S[3][1]);
            SB();
            if (c < 3) { kf0 = n0; kf1 = n1; qf = nq; }
        }
        if (grp == 0 && j < 15) sync(j);
        bf16x8 vf[2];
#pragma unroll
        for (int dt = 0; dt < 2; ++dt) vf[dt] = ldv(dt);
        SB();
        bf16x8 P[4];
        float ps[4];
        auto softmax_tile = [&]() {
#pragma unroll
            for (int nb = 0; nb < 4; ++nb) {
                f32x4 e0, e1;
#pragma unroll
                for (int r = 0; r < 4; ++r) { e0[r] = __builtin_amdgcn_exp2f(S[nb][0][r]); e1[r] = __builtin_amdgcn_exp2f(S[nb][1][r]); }
                ps[nb] = ((e0[0] + e0[1]) + (e0[2] + e0[3])) + ((e1[0] + e1[1]) + (e1[2] + e1[3]));
                P[nb] = pack8(e0, e1);
            }
        };
        softmax_tile();
        const float psm = max3f(max2f(ps[0], ps[1]), ps[2], ps[3]);
        if (j == 0 || __any(!(psm <= LIM))) {
#pragma unroll
            for (int nb = 0; nb < 4; ++nb) {
                float rm = max2f(S[nb][0][0], S[nb][0][1]);
                rm = max3f(rm, S[nb][0][2], S[nb][0][3]);
                rm = max3f(rm, S[nb][1][0], S[nb][1][1]);
                rm = max3f(rm, S[nb][1][2], S[nb][1][3]);
                rm = __builtin_fmaxf(rm, __shfl_xor(rm, 16));
                rm = __builtin_fmaxf(rm, __shfl_xor(rm, 32));
                const float delta = __builtin_fmaxf(rm, lo);
                const float alpha = __builtin_amdgcn_exp2f(__builtin_fminf(-delta, 0.f));
                negm[nb] -= delta; l[nb] *= alpha;
                S[nb][0] -= delta; S[nb][1] -= delta;
                if (nb < 3) {
#pragma unroll
                    for (int t = 0; t < 8; ++t) O[nb][t] *= alpha;
                } else { O12[0] *= alpha; O12[1] *= alpha; }
            }
            lo = 0.f;
            softmax_tile();
        }
#pragma unroll
        for (int nb = 0; nb < 4; ++nb) l[nb] += ps[nb];
        if (grp == 1 && j < 15) sync(j);
        kE = (kE + KV_STAGE) & (4 * KV_STAGE - 1);
        {
            bf16x8 va[3], vb3[3];
#pragma unroll
            for (int dt = 0; dt < 3; ++dt) va[dt] = ldv(2 + dt);
            SB();
#pragma unroll
            for (int dt = 0; dt < 2; ++dt)
#pragma unroll
                for (int nb = 0; nb < 3; ++nb) O[nb][dt] = MFMA16(vf[dt], P[nb], O[nb][dt]);
            SB();
#pragma unroll
            for (int dt = 0; dt < 3; ++dt) vb3[dt] = ldv(5 + dt);
            SB();
#pragma unroll
            for (int dt = 0; dt < 3; ++dt)
#pragma unroll
                for (int nb = 0; nb < 3; ++nb) O[nb][2 + dt] = MFMA16(va[dt], P[nb], O[nb][2 + dt]);
            SB();
            const unsigned vb = vE + wi * 512;
            const bf16x8 v12a = cat44(lds_tr(vb), lds_tr(vb + 4096)), v12b = cat44(lds_tr(vb ^ 32u), lds_tr((vb ^ 32u) + 4096));
            kf0 = ldk(0, 0); qf = lds_b128(q4a); kf1 = ldk(0, 1);
            SB();
#pragma unroll
            for (int dt = 0; dt < 3; ++dt)
#pragma unroll
                for (int nb = 0; nb < 3; ++nb) O[nb][5 + dt] = MFMA16(vb3[dt], P[nb], O[nb][5 + dt]);
            O12[0] = MFMA16(v12a, P[3], O12[0]);
            O12[1] = MFMA16(v12b, P[3], O12[1]);
            SB();
        }
        vE = (vE + KV_STAGE) & (4 * KV_STAGE - 1);
    }
    STAMP(3);
    const int ln = opaque_lane();
    asm volatile("s_waitcnt vmcnt(0)" ::: "memory");
    {
        const int G1 = (ln >> 4) & 1, Q4 = (ln & 15) >> 2;
        const unsigned bE = (unsigned)(w * 4096 + (ln >> 5) * 1024 + (4 * G1 + Q4) * 64 + 32 * G1 + 8 * (ln & 3));
#pragma unroll
        for (int nt = 0; nt < 4; ++nt) {
            const unsigned bb = ((nt & 1) ? (bE ^ 32u) : bE) + (nt >> 1) * 512;
            const bf16x8 a = cat44(lds_tr(bb), lds_tr(bb + 2048));
            *(bf16x8*)(wfp + nt * 1024 + ln * 16) = a;
        }
    }
#pragma unroll
    for (int nb = 0; nb < 4; ++nb) {
        float s = l[nb];
        s += __shfl_xor(s, 16);
        s += __shfl_xor(s, 32);
        const float inv = 1.0f / s;
        if (nb < 3) {
            char* dst = ofp + (size_t)(rb0 + nb) * 1024 + ln * 16;
#pragma unroll
            for (int c = 0; c < 4; ++c) *(bf16x8*)(dst + c * (NRB * 1024)) = pack8(O[nb][2 * c] * inv, O[nb][2 * c + 1] * inv);
        } else {
            *(bf16x8*)(ofp + (size_t)12 * 1024 + wi * (NRB * 1024) + ln * 16) = pack8(O12[0] * inv, O12[1] * inv);
        }
    }
    STAMP(4);
}

template <int NB>
__device__ __forceinline__ void oproj_body(const char* __restrict__ of, const char* __restrict__ wf, __hip_bfloat16* __restrict__ outp,
                                           char* smem, int w, int lane) {
    f32x4 acc[NB][4];
#pragma unroll
    for (int nb = 0; nb < NB; ++nb)
#pragma unroll
        for (int t = 0; t < 4; ++t) acc[nb][t] = (f32x4){0.f, 0.f, 0.f, 0.f};
    const char* op = of + (size_t)(w * 16) * (NRB * 1024) + lane * 16;
    const char* wp = wf + (size_t)(w * 16) * (32 * 1024) + lane * 16;
    bf16x8 o0[NB], w0[4], o1[NB], w1[4], o2[NB], w2[4];
    auto load = [&](bf16x8* o, bf16x8* wv, int kc) {
#pragma unroll
        for (int t = 0; t < 4; ++t) wv[t] = *(const bf16x8*)(wp + (size_t)kc * (32 * 1024) + t * 1024);
#pragma unroll
        for (int nb = 0; nb < NB; ++nb) o[nb] = *(const bf16x8*)(op + (size_t)kc * (NRB * 1024) + nb * 1024);
    };
    auto comp = [&](const bf16x8* o, const bf16x8* wv) {
#pragma unroll
        for (int nb = 0; nb < NB; ++nb)
#pragma unroll
            for (int t = 0; t < 4; ++t) acc[nb][t] = MFMA16(wv[t], o[nb], acc[nb][t]);
    };
    load(o0, w0, 0);
    load(o1, w1, 1);
#pragma clang loop unroll(disable)
    for (int kc = 0; kc < 15; kc += 3) {
        load(o2, w2, kc + 2);
        __builtin_amdgcn_sched_barrier(0);
        comp(o0, w0);
        __builtin_amdgcn_sched_barrier(0);
        load(o0, w0, kc + 3);
        __builtin_amdgcn_sched_barrier(0);
        comp(o1, w1);
        __builtin_amdgcn_sched_barrier(0);
        load(o1, w1, kc + 4 < 16 ? kc + 4 : 15);
        __builtin_amdgcn_sched_barrier(0);
        comp(o2, w2);
        __builtin_amdgcn_sched_barrier(0);
    }
    comp(o0, w0);
    f32x4* red = (f32x4*)smem;
    constexpr int NT = NB * 4;
#pragma unroll
    for (int r0 = 0; r0 < NT; r0 += 16) {
        if (r0) __syncthreads();
#pragma unroll
        for (int T = r0; T < (r0 + 16 < NT ? r0 + 16 : NT); ++T) red[((T - r0) * 8 + w) * 64 + lane] = acc[T >> 2][T & 3];
        __syncthreads();
#pragma unroll
        for (int h = 0; h < 2; ++h) {
            const int T = r0 + h * 8 + w;
            if (T < NT) {
                f32x4 s = red[((T - r0) * 8 + 0) * 64 + lane];
#pragma unroll
                for (int k = 1; k < 8; ++k) s += red[((T - r0) * 8 + k) * 64 + lane];
                const int row = (T >> 2) * 16 + (lane & 15), col = (T & 3) * 16 + (lane >> 4) * 4;
                u32x2 o = {cvtpk(s[0], s[1]), cvtpk(s[2], s[3])};
                *(u32x2*)(outp + (size_t)row * 1024 + col) = o;
            }
        }
    }
}

__global__ __launch_bounds__(512, 2) void oproj_k(const char* __restrict__ ws, __hip_bfloat16* __restrict__ out) {
    __shared__ __attribute__((aligned(16))) char smem[16 * 8 * 1024];
    const int bid = blockIdx.x, xcd = bid & 7, idx = bid >> 3;
    const int ib = 2 * xcd + (idx >> 4), half = (idx >> 3) & 1, nt = idx & 7;
    const int inst = ib >> 3, b = ib & 7;
    const int lane = threadIdx.x & 63;
    const int w = __builtin_amdgcn_readfirstlane((int)threadIdx.x >> 6);
    const int rb0 = half ? 7 : 0;
    const char* of = ws + WS_OF + ((size_t)ib * 128 * NRB + rb0) * 1024;
    const char* wf = ws + WS_WF + ((size_t)inst * 128 * 32 + nt * 4) * 1024;
    __hip_bfloat16* outp = out + (size_t)(b * SQ + rb0 * 16) * 1024 + inst * 512 + nt * 64;
    if (half) oproj_body<6>(of, wf, outp, smem, w, lane);
    else oproj_body<7>(of, wf, outp, smem, w, lane);
}

extern "C" void kernel_launch(void* const* d_in, const int* in_sizes, int n_in, void* d_out, int out_size, void* d_ws,
                              size_t ws_size, hipStream_t stream) {
    Ptrs p;
    for (int i = 0; i < 2; ++i) {
        p.x[i] = (const __hip_bfloat16*)d_in[5 * i + 0];
        p.k[i] = (const __hip_bfloat16*)d_in[5 * i + 1];
        p.v[i] = (const __hip_bfloat16*)d_in[5 * i + 2];
        p.wq[i] = (const __hip_bfloat16*)d_in[5 * i + 3];
        p.wo[i] = (const __hip_bfloat16*)d_in[5 * i + 4];
    }
    (void)hipFuncSetAttribute((const void*)attn_k, hipFuncAttributeMaxDynamicSharedMemorySize, LDS1);
    hipLaunchKernelGGL(attn_k, dim3(256), dim3(512), LDS1, stream, p, (char*)d_ws);
    hipLaunchKernelGGL(oproj_k, dim3(256), dim3(512), 0, stream, (const char*)d_ws, (__hip_bfloat16*)d_out);
}
```

```cpp
#include <hip/hip_runtime.h>
#include <hip/hip_bf16.h>

typedef __attribute__((ext_vector_type(8))) short bf16x8;
typedef __attribute__((ext_vector_type(4))) short s16x4;
typedef __attribute__((ext_vector_type(4))) float f32x4;
typedef __attribute__((ext_vector_type(2))) float f32x2;
typedef __attribute__((ext_vector_type(4))) unsigned u32x4;
typedef __attribute__((ext_vector_type(2))) unsigned u32x2;
typedef __attribute__((ext_vector_type(2))) __bf16 bf16x2_t;
typedef short v4i16_t __attribute__((ext_vector_type(4)));
#define LDS_AS __attribute__((address_space(3)))

constexpr int NB_BATCH = 8, SQ = 208, DM = 512, NH = 32, SK = 512, DH = 128;
constexpr int NRB = 13;
constexpr float QSCALE = 0.08838834764831845f * 1.4426950408889634f;
constexpr float LIM = 1024.0f;

constexpr int KV_STAGE = 32768;
constexpr int X_SLOT = 13 * 1024, W_SLOT = 16384;
constexpr int X_RING = 65536, W_RING = X_RING + 3 * X_SLOT;
constexpr int LDS1 = 4 * KV_STAGE + 16384 + 8192 + 256;

constexpr size_t WS_OF = 0;
constexpr size_t OF_BYTES = (size_t)16 * NRB * 128 * 1024;
constexpr size_t WS_WF = OF_BYTES;
constexpr size_t WF_BYTES = (size_t)2 * 128 * 32 * 1024;
constexpr size_t WS_PART = WS_WF + WF_BYTES;

#define STAMP(i)
#define STAMP_ARG
#define MFMA16(a, b, c) __builtin_amdgcn_mfma_f32_16x16x32_bf16(a, b, c, 0, 0, 0)
#define WAIT_BAR(N) asm volatile("s_waitcnt vmcnt(" #N ") lgkmcnt(0)\n\ts_barrier" ::: "memory")

__device__ __forceinline__ void glds16(const char* sbase, unsigned voff, unsigned lds_base) {
    unsigned sv;
    asm volatile("s_mov_b32 %0, m0\n\ts_mov_b32 m0, %3\n\ts_nop 0\n\tglobal_load_lds_dwordx4 %1, %2\n\ts_mov_b32 m0, %0"
                 : "=&s"(sv) : "v"(voff), "s"(sbase), "s"(lds_base) : "memory");
}
__device__ __forceinline__ void gtouch(const char* sbase, unsigned voff, unsigned lds_base) {
    unsigned sv;
    asm volatile("s_mov_b32 %0, m0\n\ts_mov_b32 m0, %3\n\ts_nop 0\n\tglobal_load_lds_dword %1, %2\n\ts_mov_b32 m0, %0"
                 : "=&s"(sv) : "v"(voff), "s"(sbase), "s"(lds_base) : "memory");
}
__device__ __forceinline__ bf16x8 lds_b128(unsigned a) { return *(const LDS_AS bf16x8*)(a); }
__device__ __forceinline__ s16x4 lds_tr(unsigned a) {
    return __builtin_bit_cast(s16x4, __builtin_amdgcn_ds_read_tr16_b64_v4i16((LDS_AS v4i16_t*)(a)));
}
__device__ __forceinline__ bf16x8 cat44(s16x4 l, s16x4 h) { return (bf16x8){l[0], l[1], l[2], l[3], h[0], h[1], h[2], h[3]}; }
__device__ __forceinline__ unsigned cvtpk(float lo, float hi) {
    f32x2 v = {lo, hi};
    return __builtin_bit_cast(unsigned, __builtin_convertvector(v, bf16x2_t));
}
__device__ __forceinline__ bf16x8 pack8(f32x4 a, f32x4 b) {
    u32x4 w = {cvtpk(a[0], a[1]), cvtpk(a[2], a[3]), cvtpk(b[0], b[1]), cvtpk(b[2], b[3])};
    return __builtin_bit_cast(bf16x8, w);
}
__device__ __forceinline__ float max3f(float a, float b, float c) { return __builtin_fmaxf(__builtin_fmaxf(a, b), c); }

struct Ptrs {
    const __hip_bfloat16* x[2];
    const __hip_bfloat16* k[2];
    const __hip_bfloat16* v[2];
    const __hip_bfloat16* wq[2];
    const __hip_bfloat16* wo[2];
};

__device__ __forceinline__ int opaque_lane() {
    int l;
    asm volatile("v_mbcnt_lo_u32_b32 %0, -1, 0\n\tv_mbcnt_hi_u32_b32 %0, -1, %0" : "=v"(l));
    return l;
}
template <int N> __device__ __forceinline__ void wait_bar() {
    asm volatile("s_waitcnt vmcnt(%0) lgkmcnt(0)\n\ts_barrier" :: "n"(N) : "memory");
}
__device__ __forceinline__ int tr_colB_of(int lane) {
    const int tr_row = (lane & 31) >> 2, cpos = lane & 3;
    return (lane >> 5) * 64 + (((cpos >> 1) ^ (tr_row >> 2)) * 32) + (cpos & 1) * 16;
}

__device__ __forceinline__ float max2f(float a, float b) { return __builtin_amdgcn_fmed3f(a, b, INFINITY); }

__global__ __launch_bounds__(512, 2) void attn_k(Ptrs p, char* __restrict__ ws) {
    const int bid = blockIdx.x, xcd = bid & 7, idx = bid >> 3;
    const int inst = xcd >> 2, hp = (xcd & 3) * 4 + (idx & 3), b = idx >> 2;
    const int w = __builtin_amdgcn_readfirstlane((int)threadIdx.x >> 6);
    const int grp = w >> 2, wi = w & 3, head = hp * 2 + grp;
    const char* xg = (const char*)(p.x[inst]) + (size_t)b * SQ * DM * 2;
    const char* wqg = (const char*)(p.wq[inst]) + (size_t)head * DH * 2;
    const char* kg = (const char*)(p.k[inst]) + (size_t)(b * NH + head) * SK * DH * 2;
    const char* vg = (const char*)(p.v[inst]) + (size_t)(b * NH + head) * SK * DH * 2;
    const char* wog = (const char*)(p.wo[inst]) + ((size_t)(hp * 256 + w * 32) * DM + b * 64) * 2;
    char* ofp = ws + WS_OF + ((size_t)(inst * 8 + b) * 128 + head * 4) * (NRB * 1024);
    char* wfp = ws + WS_WF + (((size_t)inst * 128 + hp * 8 + w) * 32 + b * 4) * 1024;
    const int rb0 = 3 * wi;
    const int rot = (int)(idx * 5 + xcd * 3) & 15;
    const int lane = threadIdx.x & 63;
    const int m = lane & 15, g = lane >> 4, q4 = m >> 2, p4 = lane & 3, hh = lane >> 5, g1 = g & 1;
    STAMP(0);
    const int tr_row = (lane & 31) >> 2;
    const int kkey = 8 * wi + (lane >> 3);
    unsigned k_lane = (unsigned)(kkey * 256 + (((lane & 7) ^ ((kkey >> 1) & 7)) * 16));
    unsigned v_lane = (unsigned)((8 * wi + tr_row) * 256 + tr_colB_of(lane));
    auto kv_one = [&](int st, int i) {
        const unsigned d = (unsigned)((st & 3) * KV_STAGE + grp * 16384);
        const int sg = (st + rot) & 15;
        if (i < 2) glds16(kg + (size_t)sg * 8192 + i * 128, k_lane, d + i * 4096 + wi * 1024);
        else glds16(vg + (size_t)sg * 8192 + (i - 2) * 128, v_lane, d + 8192 + wi * 2048 + (i - 2) * 1024);
    };
    bf16x8 Q[3][4];
    bf16x8 q12c;
    {
        const int xr = lane >> 2, xpos = lane & 3;
        const unsigned x_lane = (unsigned)(xr * (DM * 2) + ((xpos ^ ((4 - (xr >> 2)) & 3)) * 16));
        const unsigned wq_lane = (unsigned)((16 * (wi & 1) + 8 * (tr_row >> 2) + 4 * (wi >> 1) + (tr_row & 3)) * (NH * DH * 2) + tr_colB_of(lane));
        const int xb2 = (w < 5) ? 8 + w : 12;
        const char* xs1 = xg + (size_t)(w * 16) * (DM * 2);
        const char* xs2 = xg + (size_t)(xb2 * 16) * (DM * 2);
        const unsigned xd1 = (unsigned)(X_RING + w * 1024), xd2 = (unsigned)(X_RING + xb2 * 1024);
        const unsigned wd = (unsigned)(W_RING + grp * 8192 + wi * 2048);
        auto issue_xw = [&](int ks) {
            const int slot = ks % 3;
            glds16(xs1 + ks * 64, x_lane, xd1 + slot * X_SLOT);
            glds16(wqg + (size_t)ks * 32 * (NH * DH * 2), wq_lane, wd + slot * W_SLOT);
            glds16(wqg + (size_t)ks * 32 * (NH * DH * 2) + 128, wq_lane, wd + slot * W_SLOT + 1024);
            if (w < 5) glds16(xs2 + ks * 64, x_lane, xd2 + slot * X_SLOT);
        };
        const unsigned touch_lane = (unsigned)(lane & 15) * 128u;
        f32x4 acc[3][8], acc12[2];
#pragma unroll
        for (int nb = 0; nb < 3; ++nb)
#pragma unroll
            for (int t = 0; t < 8; ++t) acc[nb][t] = (f32x4){0.f, 0.f, 0.f, 0.f};
        acc12[0] = acc12[1] = (f32x4){0.f, 0.f, 0.f, 0.f};
        issue_xw(0);
        issue_xw(1);
        issue_xw(2);
        const unsigned xl = (unsigned)(X_RING + m * 64 + ((g ^ ((4 - q4) & 3)) * 16));
        const unsigned xb = xl + rb0 * 1024;
        const unsigned wb = (unsigned)(W_RING + grp * 8192 + hh * 2048 + (4 * g1 + q4) * 64 + ((16 * p4) ^ (32 * g1)));
        const unsigned wb12 = wb + wi * 512;
        bf16x8 xf[2][4], af[2][10];
        auto ldfr = [&](int ks) {
            const int slot = ks % 3, sb = ks & 1;
#pragma unroll
            for (int nb = 0; nb < 3; ++nb) xf[sb][nb] = lds_b128(xb + slot * X_SLOT + nb * 1024);
            xf[sb][3] = lds_b128(xl + slot * X_SLOT + 12 * 1024);
#pragma unroll
            for (int t = 0; t < 8; ++t)
                af[sb][t] = cat44(lds_tr(wb + slot * W_SLOT + (t >> 1) * 512 + (t & 1) * 8), lds_tr(wb + slot * W_SLOT + 4096 + (t >> 1) * 512 + (t & 1) * 8));
#pragma unroll
            for (int u = 0; u < 2; ++u)
                af[sb][8 + u] = cat44(lds_tr(wb12 + slot * W_SLOT + u * 8), lds_tr(wb12 + slot * W_SLOT + 4096 + u * 8));
        };
        if (w < 5) wait_bar<8>(); else wait_bar<6>();
        ldfr(0);
#pragma unroll
        for (int ks = 0; ks < 16; ++ks) {
            if (w < 5) { if (ks == 0) wait_bar<4>(); else if (ks <= 8) wait_bar<5>(); else if (ks <= 13) wait_bar<4>(); else if (ks == 14) wait_bar<0>(); }
            else { if (ks == 0) wait_bar<3>(); else if (ks <= 8) wait_bar<4>(); else if (ks <= 13) wait_bar<3>(); else if (ks == 14) wait_bar<0>(); }
            if (ks < 13) issue_xw(ks + 3);
            if (ks < 8) kv_one(ks >> 2, ks & 3);
            if (ks < 15) ldfr(ks + 1);
            const int sb = ks & 1;
#pragma unroll
            for (int t = 0; t < 8; ++t)
#pragma unroll
                for (int nb = 0; nb < 3; ++nb) acc[nb][t] = MFMA16(af[sb][t], xf[sb][nb], acc[nb][t]);
            acc12[0] = MFMA16(af[sb][8], xf[sb][3], acc12[0]);
            acc12[1] = MFMA16(af[sb][9], xf[sb][3], acc12[1]);
        }
#pragma unroll
        for (int nb = 0; nb < 3; ++nb)
#pragma unroll
            for (int c = 0; c < 4; ++c) Q[nb][c] = pack8(acc[nb][2 * c] * QSCALE, acc[nb][2 * c + 1] * QSCALE);
        q12c = pack8(acc12[0] * QSCALE, acc12[1] * QSCALE);
    }

    f32x4 O[3][8], O12[2];
    f32x4 negm[4];
    float l[4];
#pragma unroll
    for (int nb = 0; nb < 4; ++nb) { negm[nb] = (f32x4){0.f, 0.f, 0.f, 0.f}; l[nb] = 0.f; }
#pragma unroll
    for (int nb = 0; nb < 3; ++nb)
#pragma unroll
        for (int t = 0; t < 8; ++t) O[nb][t] = (f32x4){0.f, 0.f, 0.f, 0.f};
    O12[0] = O12[1] = (f32x4){0.f, 0.f, 0.f, 0.f};
    const int l2 = opaque_lane();
    const int m2 = l2 & 15, g2 = l2 >> 4;
    {
        const int kk2 = 8 * wi + (l2 >> 3);
        k_lane = (unsigned)(kk2 * 256 + (((l2 & 7) ^ ((kk2 >> 1) & 7)) * 16));
        v_lane = (unsigned)((8 * wi + ((l2 & 31) >> 2)) * 256 + tr_colB_of(l2));
    }
    unsigned kE = (unsigned)(grp * 16384 + m2 * 128 + ((g2 ^ (m2 >> 1)) * 16));
    unsigned vE = (unsigned)(grp * 16384 + 8192 + (l2 >> 5) * 2048 + (4 * (g2 & 1) + (m2 >> 2)) * 64 + 32 * (g2 & 1) + 8 * (l2 & 3));
    const unsigned q4a = (unsigned)(4 * KV_STAGE + 16384 + grp * 4096 + l2 * 16);
    *(LDS_AS bf16x8*)(q4a + wi * 1024) = q12c;
    STAMP(1);
#define SB() __builtin_amdgcn_sched_barrier(0)
#define P(nb) __builtin_bit_cast(bf16x8, Pw[nb])
    WAIT_BAR(0);
    STAMP(2);
#pragma unroll
    for (int i = 0; i < 4; ++i) kv_one(2, i);
    auto sync = [&](int j) {
        WAIT_BAR(4);
        if (j < 13) {
#pragma unroll
            for (int i = 0; i < 4; ++i) kv_one(j + 3, i);
        } else if (j == 13) {
            const int ln = opaque_lane();
            const unsigned wo_lane = (unsigned)(((ln & 31) >> 2) * (DM * 2) + tr_colB_of(ln));
#pragma unroll
            for (int i = 0; i < 4; ++i) glds16(wog + (size_t)(8 * i) * (DM * 2), wo_lane, (unsigned)(w * 4096 + i * 1024));
        }
    };
    auto ldk = [&](int c, int kt) { return lds_b128(((c & 1) ? (kE ^ 64u) : kE) + (c >> 1) * 4096 + kt * 2048); };
    auto ldv = [&](int dt) {
        const unsigned vb = ((dt & 1) ? (vE ^ 32u) : vE) + (dt >> 1) * 512;
        return cat44(lds_tr(vb), lds_tr(vb + 4096));
    };
#define PRIO 1
    if (PRIO == 1 && grp == 1) __builtin_amdgcn_s_setprio(1);
    if (PRIO == 2 && grp == 0) __builtin_amdgcn_s_setprio(1);
    float lo = -INFINITY;
    bf16x8 Q3[4];
#pragma unroll
    for (int c = 0; c < 4; ++c) Q3[c] = lds_b128(q4a + c * 1024);
    bf16x8 kf0 = ldk(0, 0), kf1 = ldk(1, 0);
#pragma clang loop unroll(disable)
    for (int j = 0; j < 16; ++j) {
        STAMP(8 + j);
        if (grp == 0 && j < 15) sync(j);
        f32x4 S[4][2];
        u32x4 Pw[4];
        float ps[4];
        auto qk_half = [&](int kt, bool pre) {
#pragma unroll
            for (int c = 0; c < 4; ++c) {
                const bf16x8 a = (pre && c == 0) ? kf0 : (pre && c == 1) ? kf1 : ldk(c, kt);
#pragma unroll
                for (int nb = 0; nb < 3; ++nb) S[nb][kt] = MFMA16(a, Q[nb][c], c == 0 ? negm[nb] : S[nb][kt]);
                S[3][kt] = MFMA16(a, Q3[c], c == 0 ? negm[3] : S[3][kt]);
            }
        };
        auto sm_half = [&](int kt) {
#pragma unroll
            for (int nb = 0; nb < 4; ++nb) {
                f32x4 e;
#pragma unroll
                for (int r = 0; r < 4; ++r) e[r] = __builtin_amdgcn_exp2f(S[nb][kt][r]);
                const float t = (e[0] + e[1]) + (e[2] + e[3]);
                ps[nb] = kt ? ps[nb] + t : t;
                Pw[nb][2 * kt] = cvtpk(e[0], e[1]);
                Pw[nb][2 * kt + 1] = cvtpk(e[2], e[3]);
            }
        };
        qk_half(0, true);
        SB();
        qk_half(1, false);
        sm_half(0);
        SB();
        sm_half(1);
        const float psm = max3f(max2f(ps[0], ps[1]), ps[2], ps[3]);
        if (j == 0 || __any(!(psm <= LIM))) {
            qk_half(0, false);
            qk_half(1, false);
#pragma unroll
            for (int nb = 0; nb < 4; ++nb) {
                float rm = max2f(S[nb][0][0], S[nb][0][1]);
                rm = max3f(rm, S[nb][0][2], S[nb][0][3]);
                rm = max3f(rm, S[nb][1][0], S[nb][1][1]);
                rm = max3f(rm, S[nb][1][2], S[nb][1][3]);
                rm = __builtin_fmaxf(rm, __shfl_xor(rm, 16));
                rm = __builtin_fmaxf(rm, __shfl_xor(rm, 32));
                const float delta = __builtin_fmaxf(rm, lo);
                const float alpha = __builtin_amdgcn_exp2f(__builtin_fminf(-delta, 0.f));
                negm[nb] -= delta; l[nb] *= alpha;
                S[nb][0] -= delta; S[nb][1] -= delta;
                if (nb < 3) {
#pragma unroll
                    for (int t = 0; t < 8; ++t) O[nb][t] *= alpha;
                } else { O12[0] *= alpha; O12[1] *= alpha; }
            }
            lo = 0.f;
            sm_half(0);
            sm_half(1);
        }
#pragma unroll
        for (int nb = 0; nb < 4; ++nb) l[nb] += ps[nb];
        if (grp == 1 && j < 15) sync(j);
        kE = (kE + KV_STAGE) & (4 * KV_STAGE - 1);
        {
            bf16x8 vf[2], va[3], vb3[3];
#pragma unroll
            for (int dt = 0; dt < 2; ++dt) vf[dt] = ldv(dt);
#pragma unroll
            for (int dt = 0; dt < 3; ++dt) va[dt] = ldv(2 + dt);
            SB();
#pragma unroll
            for (int dt = 0; dt < 2; ++dt)
#pragma unroll
                for (int nb = 0; nb < 3; ++nb) O[nb][dt] = MFMA16(vf[dt], P(nb), O[nb][dt]);
            SB();
#pragma unroll
            for (int dt = 0; dt < 3; ++dt) vb3[dt] = ldv(5 + dt);
            SB();
#pragma unroll
            for (int dt = 0; dt < 3; ++dt)
#pragma unroll
                for (int nb = 0; nb < 3; ++nb) O[nb][2 + dt] = MFMA16(va[dt], P(nb), O[nb][2 + dt]);
            SB();
            const unsigned vb = vE + wi * 512;
            const bf16x8 v12a = cat44(lds_tr(vb), lds_tr(vb + 4096)), v12b = cat44(lds_tr(vb ^ 32u), lds_tr((vb ^ 32u) + 4096));
            kf0 = ldk(0, 0); kf1 = ldk(1, 0);
            SB();
#pragma unroll
            for (int dt = 0; dt < 3; ++dt)
#pragma unroll
                for (int nb = 0; nb < 3; ++nb) O[nb][5 + dt] = MFMA16(vb3[dt], P(nb), O[nb][5 + dt]);
            O12[0] = MFMA16(v12a, P(3), O12[0]);
            O12[1] = MFMA16(v12b, P(3), O12[1]);
            SB();
        }
        vE = (vE + KV_STAGE) & (4 * KV_STAGE - 1);
    }
    STAMP(3);
    const int ln = opaque_lane();
    asm volatile("s_waitcnt vmcnt(0)" ::: "memory");
    {
        const int G1 = (ln >> 4) & 1, Q4 = (ln & 15) >> 2;
        const unsigned bE = (unsigned)(w * 4096 + (ln >> 5) * 1024 + (4 * G1 + Q4) * 64 + 32 * G1 + 8 * (ln & 3));
#pragma unroll
        for (int nt = 0; nt < 4; ++nt) {
            const unsigned bb = ((nt & 1) ? (bE ^ 32u) : bE) + (nt >> 1) * 512;
            const bf16x8 a = cat44(lds_tr(bb), lds_tr(bb + 2048));
            *(bf16x8*)(wfp + nt * 1024 + ln * 16) = a;
        }
    }
#pragma unroll
    for (int nb = 0; nb < 4; ++nb) {
        float s = l[nb];
        s += __shfl_xor(s, 16);
        s += __shfl_xor(s, 32);
        const float inv = 1.0f / s;
        if (nb < 3) {
            char* dst = ofp + (size_t)(rb0 + nb) * 1024 + ln * 16;
#pragma unroll
            for (int c = 0; c < 4; ++c) *(bf16x8*)(dst + c * (NRB * 1024)) = pack8(O[nb][2 * c] * inv, O[nb][2 * c + 1] * inv);
        } else {
            *(bf16x8*)(ofp + (size_t)12 * 1024 + wi * (NRB * 1024) + ln * 16) = pack8(O12[0] * inv, O12[1] * inv);
        }
    }
    STAMP(4);
}

template <int NB>
__device__ __forceinline__ void oproj_body(const char* __restrict__ of, const char* __restrict__ wf, __hip_bfloat16* __restrict__ outp,
                                           char* smem, int w, int lane) {
    f32x4 acc[NB][4];
#pragma unroll
    for (int nb = 0; nb < NB; ++nb)
#pragma unroll
        for (int t = 0; t < 4; ++t) acc[nb][t] = (f32x4){0.f, 0.f, 0.f, 0.f};
    const char* op = of + (size_t)(w * 16) * (NRB * 1024) + lane * 16;
    const char* wp = wf + (size_t)(w * 16) * (32 * 1024) + lane * 16;
    bf16x8 o0[NB], w0[4], o1[NB], w1[4], o2[NB], w2[4];
    auto load = [&](bf16x8* o, bf16x8* wv, int kc) {
#pragma unroll
        for (int t = 0; t < 4; ++t) wv[t] = *(const bf16x8*)(wp + (size_t)kc * (32 * 1024) + t * 1024);
#pragma unroll
        for (int nb = 0; nb < NB; ++nb) o[nb] = *(const bf16x8*)(op + (size_t)kc * (NRB * 1024) + nb * 1024);
    };
    auto comp = [&](const bf16x8* o, const bf16x8* wv) {
#pragma unroll
        for (int nb = 0; nb < NB; ++nb)
#pragma unroll
            for (int t = 0; t < 4; ++t) acc[nb][t] = MFMA16(wv[t], o[nb], acc[nb][t]);
    };
    load(o0, w0, 0);
    load(o1, w1, 1);
#pragma clang loop unroll(disable)
    for (int kc = 0; kc < 15; kc += 3) {
        load(o2, w2, kc + 2);
        __builtin_amdgcn_sched_barrier(0);
        comp(o0, w0);
        __builtin_amdgcn_sched_barrier(0);
        load(o0, w0, kc + 3);
        __builtin_amdgcn_sched_barrier(0);
        comp(o1, w1);
        __builtin_amdgcn_sched_barrier(0);
        load(o1, w1, kc + 4 < 16 ? kc + 4 : 15);
        __builtin_amdgcn_sched_barrier(0);
        comp(o2, w2);
        __builtin_amdgcn_sched_barrier(0);
    }
    comp(o0, w0);
    f32x4* red = (f32x4*)smem;
    constexpr int NT = NB * 4;
#pragma unroll
    for (int r0 = 0; r0 < NT; r0 += 16) {
        if (r0) __syncthreads();
#pragma unroll
        for (int T = r0; T < (r0 + 16 < NT ? r0 + 16 : NT); ++T) red[((T - r0) * 8 + w) * 64 + lane] = acc[T >> 2][T & 3];
        __syncthreads();
#pragma unroll
        for (int h = 0; h < 2; ++h) {
            const int T = r0 + h * 8 + w;
            if (T < NT) {
                f32x4 s = red[((T - r0) * 8 + 0) * 64 + lane];
#pragma unroll
                for (int k = 1; k < 8; ++k) s += red[((T - r0) * 8 + k) * 64 + lane];
                const int row = (T >> 2) * 16 + (lane & 15), col = (T & 3) * 16 + (lane >> 4) * 4;
                u32x2 o = {cvtpk(s[0], s[1]), cvtpk(s[2], s[3])};
                *(u32x2*)(outp + (size_t)row * 1024 + col) = o;
            }
        }
    }
}

__global__ __launch_bounds__(512, 2) void oproj_k(const char* __restrict__ ws, __hip_bfloat16* __restrict__ out) {
    __shared__ __attribute__((aligned(16))) char smem[16 * 8 * 1024];
    const int bid = blockIdx.x, xcd = bid & 7, idx = bid >> 3;
    const int ib = 2 * xcd + (idx >> 4), half = (idx >> 3) & 1, nt = idx & 7;
    const int inst = ib >> 3, b = ib & 7;
    const int lane = threadIdx.x & 63;
    const int w = __builtin_amdgcn_readfirstlane((int)threadIdx.x >> 6);
    const int rb0 = half ? 7 : 0;
    const char* of = ws + WS_OF + ((size_t)ib * 128 * NRB + rb0) * 1024;
    const char* wf = ws + WS_WF + ((size_t)inst * 128 * 32 + nt * 4) * 1024;
    __hip_bfloat16* outp = out + (size_t)(b * SQ + rb0 * 16) * 1024 + inst * 512 + nt * 64;
    if (half) oproj_body<6>(of, wf, outp, smem, w, lane);
    else oproj_body<7>(of, wf, outp, smem, w, lane);
}

extern "C" void kernel_launch(void* const* d_in, const int* in_sizes, int n_in, void* d_out, int out_size, void* d_ws,
                              size_t ws_size, hipStream_t stream) {
    Ptrs p;
    for (int i = 0; i < 2; ++i) {
        p.x[i] = (const __hip_bfloat16*)d_in[5 * i + 0];
        p.k[i] = (const __hip_bfloat16*)d_in[5 * i + 1];
        p.v[i] = (const __hip_bfloat16*)d_in[5 * i + 2];
        p.wq[i] = (const __hip_bfloat16*)d_in[5 * i + 3];
        p.wo[i] = (const __hip_bfloat16*)d_in[5 * i + 4];
    }
    (void)hipFuncSetAttribute((const void*)attn_k, hipFuncAttributeMaxDynamicSharedMemorySize, LDS1);
    hipLaunchKernelGGL(attn_k, dim3(256), dim3(512), LDS1, stream, p, (char*)d_ws);
    hipLaunchKernelGGL(oproj_k, dim3(256), dim3(512), 0, stream, (const char*)d_ws, (__hip_bfloat16*)d_out);
}
```

```cpp
#include <hip/hip_runtime.h>
#include <hip/hip_bf16.h>

typedef __attribute__((ext_vector_type(8))) short bf16x8;
typedef __attribute__((ext_vector_type(4))) short s16x4;
typedef __attribute__((ext_vector_type(4))) float f32x4;
typedef __attribute__((ext_vector_type(2))) float f32x2;
typedef __attribute__((ext_vector_type(4))) unsigned u32x4;
typedef __attribute__((ext_vector_type(2))) unsigned u32x2;
typedef __attribute__((ext_vector_type(2))) __bf16 bf16x2_t;
typedef short v4i16_t __attribute__((ext_vector_type(4)));
#define LDS_AS __attribute__((address_space(3)))

constexpr int NB_BATCH = 8, SQ = 208, DM = 512, NH = 32, SK = 512, DH = 128;
constexpr int NRB = 13;
constexpr float QSCALE = 0.08838834764831845f * 1.4426950408889634f;
constexpr float LIM = 1024.0f;

constexpr int KV_STAGE = 32768;
constexpr int X_SLOT = 13 * 1024, W_SLOT = 16384;
constexpr int X_RING = 65536, W_RING = X_RING + 3 * X_SLOT;
constexpr int LDS1 = 4 * KV_STAGE + 16384 + 8192;

constexpr size_t WS_OF = 0;
constexpr size_t OF_BYTES = (size_t)16 * NRB * 128 * 1024;
constexpr size_t WS_WF = OF_BYTES;
constexpr size_t WF_BYTES = (size_t)2 * 128 * 32 * 1024;

#define STAMP(i)
#define STAMP_ARG
#define MFMA16(a, b, c) __builtin_amdgcn_mfma_f32_16x16x32_bf16(a, b, c, 0, 0, 0)
#define WAIT_BAR(N) asm volatile("s_waitcnt vmcnt(" #N ") lgkmcnt(0)\n\ts_barrier" ::: "memory")

__device__ __forceinline__ void glds16(const char* sbase, unsigned voff, unsigned lds_base) {
    asm volatile("s_mov_b32 m0, %2\n\ts_nop 0\n\tglobal_load_lds_dwordx4 %0, %1"
                 :: "v"(voff), "s"(sbase), "s"(lds_base) : "memory");
}
__device__ __forceinline__ bf16x8 lds_b128(unsigned a) { return *(const LDS_AS bf16x8*)(a); }
__device__ __forceinline__ s16x4 lds_tr(unsigned a) {
    return __builtin_bit_cast(s16x4, __builtin_amdgcn_ds_read_tr16_b64_v4i16((LDS_AS v4i16_t*)(a)));
}
__device__ __forceinline__ bf16x8 cat44(s16x4 l, s16x4 h) { return (bf16x8){l[0], l[1], l[2], l[3], h[0], h[1], h[2], h[3]}; }
__device__ __forceinline__ unsigned cvtpk(float lo, float hi) {
    f32x2 v = {lo, hi};
    return __builtin_bit_cast(unsigned, __builtin_convertvector(v, bf16x2_t));
}
__device__ __forceinline__ bf16x8 pack8(f32x4 a, f32x4 b) {
    u32x4 w = {cvtpk(a[0], a[1]), cvtpk(a[2], a[3]), cvtpk(b[0], b[1]), cvtpk(b[2], b[3])};
    return __builtin_bit_cast(bf16x8, w);
}
__device__ __forceinline__ float max3f(float a, float b, float c) { return __builtin_fmaxf(__builtin_fmaxf(a, b), c); }

struct Ptrs {
    const __hip_bfloat16* x[2];
    const __hip_bfloat16* k[2];
    const __hip_bfloat16* v[2];
    const __hip_bfloat16* wq[2];
    const __hip_bfloat16* wo[2];
};

__device__ __forceinline__ int opaque_lane() {
    int l;
    asm volatile("v_mbcnt_lo_u32_b32 %0, -1, 0\n\tv_mbcnt_hi_u32_b32 %0, -1, %0" : "=v"(l));
    return l;
}
template <int N> __device__ __forceinline__ void wait_bar() {
    asm volatile("s_waitcnt vmcnt(%0) lgkmcnt(0)\n\ts_barrier" :: "n"(N) : "memory");
}
__device__ __forceinline__ int tr_colB_of(int lane) {
    const int tr_row = (lane & 31) >> 2, cpos = lane & 3;
    return (lane >> 5) * 64 + (((cpos >> 1) ^ (tr_row >> 2)) * 32) + (cpos & 1) * 16;
}

__device__ __forceinline__ float max2f(float a, float b) { return __builtin_amdgcn_fmed3f(a, b, INFINITY); }

__global__ __launch_bounds__(512, 2) void attn_k(Ptrs p, char* __restrict__ ws) {
    const int bid = blockIdx.x, xcd = bid & 7, idx = bid >> 3;
    const int inst = xcd >> 2, hp = (xcd & 3) * 4 + (idx & 3), b = idx >> 2;
    const int w = __builtin_amdgcn_readfirstlane((int)threadIdx.x >> 6);
    const int grp = w >> 2, wi = w & 3, head = hp * 2 + grp;
    const char* xg = (const char*)(p.x[inst]) + (size_t)b * SQ * DM * 2;
    const char* wqg = (const char*)(p.wq[inst]) + (size_t)head * DH * 2;
    const char* kg = (const char*)(p.k[inst]) + (size_t)(b * NH + head) * SK * DH * 2;
    const char* vg = (const char*)(p.v[inst]) + (size_t)(b * NH + head) * SK * DH * 2;
    const char* wog = (const char*)(p.wo[inst]) + ((size_t)(hp * 256 + w * 32) * DM + b * 64) * 2;
    char* ofp = ws + WS_OF + ((size_t)(inst * 8 + b) * 128 + head * 4) * (NRB * 1024);
    char* wfp = ws + WS_WF + (((size_t)inst * 128 + hp * 8 + w) * 32 + b * 4) * 1024;
    const int rb0 = 3 * wi;
    const int rot = (int)(idx * 5 + xcd * 3) & 15;
    const int lane = threadIdx.x & 63;
    const int m = lane & 15, g = lane >> 4, q4 = m >> 2, p4 = lane & 3, hh = lane >> 5, g1 = g & 1;
    STAMP(0);
    const int tr_row = (lane & 31) >> 2;
    const int kkey = 8 * wi + (lane >> 3);
    unsigned k_lane = (unsigned)(kkey * 256 + (((lane & 7) ^ ((kkey >> 1) & 7)) * 16));
    unsigned v_lane = (unsigned)((8 * wi + tr_row) * 256 + tr_colB_of(lane));
    auto kv_one = [&](int st, int i) {
        const unsigned d = (unsigned)((st & 3) * KV_STAGE + grp * 16384);
        const int sg = (st + rot) & 15;
        if (i < 2) glds16(kg + (size_t)sg * 8192 + i * 128, k_lane, d + i * 4096 + wi * 1024);
        else glds16(vg + (size_t)sg * 8192 + (i - 2) * 128, v_lane, d + 8192 + wi * 2048 + (i - 2) * 1024);
    };
    bf16x8 Q[3][4];
    bf16x8 q12c;
    {
        const int xr = lane >> 2, xpos = lane & 3;
        const unsigned x_lane = (unsigned)(xr * (DM * 2) + ((xpos ^ ((4 - (xr >> 2)) & 3)) * 16));
        const unsigned wq_lane = (unsigned)((16 * (wi & 1) + 8 * (tr_row >> 2) + 4 * (wi >> 1) + (tr_row & 3)) * (NH * DH * 2) + tr_colB_of(lane));
        const int xb2 = (w < 5) ? 8 + w : 12;
        const char* xs1 = xg + (size_t)(w * 16) * (DM * 2);
        const char* xs2 = xg + (size_t)(xb2 * 16) * (DM * 2);
        const unsigned xd1 = (unsigned)(X_RING + w * 1024), xd2 = (unsigned)(X_RING + xb2 * 1024);
        const unsigned wd = (unsigned)(W_RING + grp * 8192 + wi * 2048);
        auto issue_xw = [&](int ks) {
            const int slot = ks % 3;
            glds16(xs1 + ks * 64, x_lane, xd1 + slot * X_SLOT);
            glds16(wqg + (size_t)ks * 32 * (NH * DH * 2), wq_lane, wd + slot * W_SLOT);
            glds16(wqg + (size_t)ks * 32 * (NH * DH * 2) + 128, wq_lane, wd + slot * W_SLOT + 1024);
            if (w < 5) glds16(xs2 + ks * 64, x_lane, xd2 + slot * X_SLOT);
        };
        f32x4 acc[3][8], acc12[2];
#pragma unroll
        for (int nb = 0; nb < 3; ++nb)
#pragma unroll
            for (int t = 0; t < 8; ++t) acc[nb][t] = (f32x4){0.f, 0.f, 0.f, 0.f};
        acc12[0] = acc12[1] = (f32x4){0.f, 0.f, 0.f, 0.f};
        issue_xw(0);
        issue_xw(1);
        issue_xw(2);
        const unsigned xl = (unsigned)(X_RING + m * 64 + ((g ^ ((4 - q4) & 3)) * 16));
        const unsigned xb = xl + rb0 * 1024;
        const unsigned wb = (unsigned)(W_RING + grp * 8192 + hh * 2048 + (4 * g1 + q4) * 64 + ((16 * p4) ^ (32 * g1)));
        const unsigned wb12 = wb + wi * 512;
        bf16x8 xf[2][4], af[2][10];
        auto ldfr = [&](int ks) {
            const int slot = ks % 3, sb = ks & 1;
#pragma unroll
            for (int nb = 0; nb < 3; ++nb) xf[sb][nb] = lds_b128(xb + slot * X_SLOT + nb * 1024);
            xf[sb][3] = lds_b128(xl + slot * X_SLOT + 12 * 1024);
#pragma unroll
            for (int t = 0; t < 8; ++t)
                af[sb][t] = cat44(lds_tr(wb + slot * W_SLOT + (t >> 1) * 512 + (t & 1) * 8), lds_tr(wb + slot * W_SLOT + 4096 + (t >> 1) * 512 + (t & 1) * 8));
#pragma unroll
            for (int u = 0; u < 2; ++u)
                af[sb][8 + u] = cat44(lds_tr(wb12 + slot * W_SLOT + u * 8), lds_tr(wb12 + slot * W_SLOT + 4096 + u * 8));
        };
        if (w < 5) wait_bar<8>(); else wait_bar<6>();
        ldfr(0);
#pragma unroll
        for (int ks = 0; ks < 16; ++ks) {
            if (w < 5) { if (ks == 0) wait_bar<4>(); else if (ks <= 8) wait_bar<5>(); else if (ks <= 13) wait_bar<4>(); else if (ks == 14) wait_bar<0>(); }
            else { if (ks == 0) wait_bar<3>(); else if (ks <= 8) wait_bar<4>(); else if (ks <= 13) wait_bar<3>(); else if (ks == 14) wait_bar<0>(); }
            if (ks < 13) issue_xw(ks + 3);
            if (ks < 8) kv_one(ks >> 2, ks & 3);
            if (ks < 15) ldfr(ks + 1);
            const int sb = ks & 1;
#pragma unroll
            for (int t = 0; t < 8; ++t)
#pragma unroll
                for (int nb = 0; nb < 3; ++nb) acc[nb][t] = MFMA16(af[sb][t], xf[sb][nb], acc[nb][t]);
            acc12[0] = MFMA16(af[sb][8], xf[sb][3], acc12[0]);
            acc12[1] = MFMA16(af[sb][9], xf[sb][3], acc12[1]);
        }
#pragma unroll
        for (int nb = 0; nb < 3; ++nb)
#pragma unroll
            for (int c = 0; c < 4; ++c) Q[nb][c] = pack8(acc[nb][2 * c] * QSCALE, acc[nb][2 * c + 1] * QSCALE);
        q12c = pack8(acc12[0] * QSCALE, acc12[1] * QSCALE);
    }

    f32x4 O[3][8], O12[2];
    f32x4 negm[4];
    float l[4];
#pragma unroll
    for (int nb = 0; nb < 4; ++nb) { negm[nb] = (f32x4){0.f, 0.f, 0.f, 0.f}; l[nb] = 0.f; }
#pragma unroll
    for (int nb = 0; nb < 3; ++nb)
#pragma unroll
        for (int t = 0; t < 8; ++t) O[nb][t] = (f32x4){0.f, 0.f, 0.f, 0.f};
    O12[0] = O12[1] = (f32x4){0.f, 0.f, 0.f, 0.f};
    const int l2 = opaque_lane();
    const int m2 = l2 & 15, g2 = l2 >> 4;
    {
        const int kk2 = 8 * wi + (l2 >> 3);
        k_lane = (unsigned)(kk2 * 256 + (((l2 & 7) ^ ((kk2 >> 1) & 7)) * 16));
        v_lane = (unsigned)((8 * wi + ((l2 & 31) >> 2)) * 256 + tr_colB_of(l2));
    }
    unsigned kE = (unsigned)(grp * 16384 + m2 * 128 + ((g2 ^ (m2 >> 1)) * 16));
    unsigned vE = (unsigned)(grp * 16384 + 8192 + (l2 >> 5) * 2048 + (4 * (g2 & 1) + (m2 >> 2)) * 64 + 32 * (g2 & 1) + 8 * (l2 & 3));
    const unsigned q4a = (unsigned)(4 * KV_STAGE + 16384 + grp * 4096 + l2 * 16);
    *(LDS_AS bf16x8*)(q4a + wi * 1024) = q12c;
    STAMP(1);
#define SB() __builtin_amdgcn_sched_barrier(0)
#define P(nb) __builtin_bit_cast(bf16x8, Pw[nb])
    WAIT_BAR(0);
    STAMP(2);
#pragma unroll
    for (int i = 0; i < 4; ++i) kv_one(2, i);
    auto sync = [&](int j) {
        WAIT_BAR(4);
        if (j < 13) {
#pragma unroll
            for (int i = 0; i < 4; ++i) kv_one(j + 3, i);
        } else if (j == 13) {
            const int ln = opaque_lane();
            const unsigned wo_lane = (unsigned)(((ln & 31) >> 2) * (DM * 2) + tr_colB_of(ln));
#pragma unroll
            for (int i = 0; i < 4; ++i) glds16(wog + (size_t)(8 * i) * (DM * 2), wo_lane, (unsigned)(w * 4096 + i * 1024));
        }
    };
    auto ldk = [&](int c, int kt) { return lds_b128(((c & 1) ? (kE ^ 64u) : kE) + (c >> 1) * 4096 + kt * 2048); };
    auto ldv = [&](int dt) {
        const unsigned vb = ((dt & 1) ? (vE ^ 32u) : vE) + (dt >> 1) * 512;
        return cat44(lds_tr(vb), lds_tr(vb + 4096));
    };
#define PRIO 1
    if (PRIO == 1 && grp == 1) __builtin_amdgcn_s_setprio(1);
    if (PRIO == 2 && grp == 0) __builtin_amdgcn_s_setprio(1);
    float lo = -INFINITY;
    bf16x8 Q3[4];
#pragma unroll
    for (int c = 0; c < 4; ++c) Q3[c] = lds_b128(q4a + c * 1024);
    bf16x8 kf0 = ldk(0, 0), kf1 = ldk(1, 0);
#pragma clang loop unroll(disable)
    for (int j = 0; j < 16; ++j) {
        STAMP(8 + j);
        if (grp == 0 && j < 15) sync(j);
        f32x4 S[4][2];
        u32x4 Pw[4];
        float ps[4];
        auto qk_half = [&](int kt, bool pre) {
#pragma unroll
            for (int c = 0; c < 4; ++c) {
                const bf16x8 a = (pre && c == 0) ? kf0 : (pre && c == 1) ? kf1 : ldk(c, kt);
#pragma unroll
                for (int nb = 0; nb < 3; ++nb) S[nb][kt] = MFMA16(a, Q[nb][c], c == 0 ? negm[nb] : S[nb][kt]);
                S[3][kt] = MFMA16(a, Q3[c], c == 0 ? negm[3] : S[3][kt]);
            }
        };
        auto sm_half = [&](int kt) {
#pragma unroll
            for (int nb = 0; nb < 4; ++nb) {
                f32x4 e;
#pragma unroll
                for (int r = 0; r < 4; ++r) e[r] = __builtin_amdgcn_exp2f(S[nb][kt][r]);
                const float t = (e[0] + e[1]) + (e[2] + e[3]);
                ps[nb] = kt ? ps[nb] + t : t;
                Pw[nb][2 * kt] = cvtpk(e[0], e[1]);
                Pw[nb][2 * kt + 1] = cvtpk(e[2], e[3]);
            }
        };
        qk_half(0, true);
        SB();
        qk_half(1, false);
        sm_half(0);
        SB();
        sm_half(1);
        const float psm = max3f(max2f(ps[0], ps[1]), ps[2], ps[3]);
        if (j == 0 || __any(!(psm <= LIM))) {
            qk_half(0, false);
            qk_half(1, false);
#pragma unroll
            for (int nb = 0; nb < 4; ++nb) {
                float rm = max2f(S[nb][0][0], S[nb][0][1]);
                rm = max3f(rm, S[nb][0][2], S[nb][0][3]);
                rm = max3f(rm, S[nb][1][0], S[nb][1][1]);
                rm = max3f(rm, S[nb][1][2], S[nb][1][3]);
                rm = __builtin_fmaxf(rm, __shfl_xor(rm, 16));
                rm = __builtin_fmaxf(rm, __shfl_xor(rm, 32));
                const float delta = __builtin_fmaxf(rm, lo);
                const float alpha = __builtin_amdgcn_exp2f(__builtin_fminf(-delta, 0.f));
                negm[nb] -= delta; l[nb] *= alpha;
                S[nb][0] -= delta; S[nb][1] -= delta;
                if (nb < 3) {
#pragma unroll
                    for (int t = 0; t < 8; ++t) O[nb][t] *= alpha;
                } else { O12[0] *= alpha; O12[1] *= alpha; }
            }
            lo = 0.f;
            sm_half(0);
            sm_half(1);
        }
#pragma unroll
        for (int nb = 0; nb < 4; ++nb) l[nb] += ps[nb];
        if (grp == 1 && j < 15) sync(j);
        kE = (kE + KV_STAGE) & (4 * KV_STAGE - 1);
        {
            bf16x8 vf[2], va[3], vb3[3];
#pragma unroll
            for (int dt = 0; dt < 2; ++dt) vf[dt] = ldv(dt);
#pragma unroll
            for (int dt = 0; dt < 3; ++dt) va[dt] = ldv(2 + dt);
            SB();
#pragma unroll
            for (int dt = 0; dt < 2; ++dt)
#pragma unroll
                for (int nb = 0; nb < 3; ++nb) O[nb][dt] = MFMA16(vf[dt], P(nb), O[nb][dt]);
            SB();
#pragma unroll
            for (int dt = 0; dt < 3; ++dt) vb3[dt] = ldv(5 + dt);
            SB();
#pragma unroll
            for (int dt = 0; dt < 3; ++dt)
#pragma unroll
                for (int nb = 0; nb < 3; ++nb) O[nb][2 + dt] = MFMA16(va[dt], P(nb), O[nb][2 + dt]);
            SB();
            const unsigned vb = vE + wi * 512;
            const bf16x8 v12a = cat44(lds_tr(vb), lds_tr(vb + 4096)), v12b = cat44(lds_tr(vb ^ 32u), lds_tr((vb ^ 32u) + 4096));
            kf0 = ldk(0, 0); kf1 = ldk(1, 0);
            SB();
#pragma unroll
            for (int dt = 0; dt < 3; ++dt)
#pragma unroll
                for (int nb = 0; nb < 3; ++nb) O[nb][5 + dt] = MFMA16(vb3[dt], P(nb), O[nb][5 + dt]);
            O12[0] = MFMA16(v12a, P(3), O12[0]);
            O12[1] = MFMA16(v12b, P(3), O12[1]);
            SB();
        }
        vE = (vE + KV_STAGE) & (4 * KV_STAGE - 1);
    }
    STAMP(3);
    const int ln = opaque_lane();
    asm volatile("s_waitcnt vmcnt(0)" ::: "memory");
    {
        const int G1 = (ln >> 4) & 1, Q4 = (ln & 15) >> 2;
        const unsigned bE = (unsigned)(w * 4096 + (ln >> 5) * 1024 + (4 * G1 + Q4) * 64 + 32 * G1 + 8 * (ln & 3));
#pragma unroll
        for (int nt = 0; nt < 4; ++nt) {
            const unsigned bb = ((nt & 1) ? (bE ^ 32u) : bE) + (nt >> 1) * 512;
            const bf16x8 a = cat44(lds_tr(bb), lds_tr(bb + 2048));
            *(bf16x8*)(wfp + nt * 1024 + ln * 16) = a;
        }
    }
#pragma unroll
    for (int nb = 0; nb < 4; ++nb) {
        float s = l[nb];
        s += __shfl_xor(s, 16);
        s += __shfl_xor(s, 32);
        const float inv = 1.0f / s;
        if (nb < 3) {
            char* dst = ofp + (size_t)(rb0 + nb) * 1024 + ln * 16;
#pragma unroll
            for (int c = 0; c < 4; ++c) *(bf16x8*)(dst + c * (NRB * 1024)) = pack8(O[nb][2 * c] * inv, O[nb][2 * c + 1] * inv);
        } else {
            *(bf16x8*)(ofp + (size_t)12 * 1024 + wi * (NRB * 1024) + ln * 16) = pack8(O12[0] * inv, O12[1] * inv);
        }
    }
    STAMP(4);
}

template <int NB>
__device__ __forceinline__ void oproj_body(const char* __restrict__ of, const char* __restrict__ wf, __hip_bfloat16* __restrict__ outp,
                                           char* smem, int w, int lane) {
    f32x4 acc[NB][4];
#pragma unroll
    for (int nb = 0; nb < NB; ++nb)
#pragma unroll
        for (int t = 0; t < 4; ++t) acc[nb][t] = (f32x4){0.f, 0.f, 0.f, 0.f};
    const char* op = of + (size_t)(w * 16) * (NRB * 1024) + lane * 16;
    const char* wp = wf + (size_t)(w * 16) * (32 * 1024) + lane * 16;
    bf16x8 o0[NB], w0[4], o1[NB], w1[4], o2[NB], w2[4];
    auto load = [&](bf16x8* o, bf16x8* wv, int kc) {
#pragma unroll
        for (int t = 0; t < 4; ++t) wv[t] = *(const bf16x8*)(wp + (size_t)kc * (32 * 1024) + t * 1024);
#pragma unroll
        for (int nb = 0; nb < NB; ++nb) o[nb] = *(const bf16x8*)(op + (size_t)kc * (NRB * 1024) + nb * 1024);
    };
    auto comp = [&](const bf16x8* o, const bf16x8* wv) {
#pragma unroll
        for (int nb = 0; nb < NB; ++nb)
#pragma unroll
            for (int t = 0; t < 4; ++t) acc[nb][t] = MFMA16(wv[t], o[nb], acc[nb][t]);
    };
    load(o0, w0, 0);
    load(o1, w1, 1);
#pragma clang loop unroll(disable)
    for (int kc = 0; kc < 15; kc += 3) {
        load(o2, w2, kc + 2);
        __builtin_amdgcn_sched_barrier(0);
        comp(o0, w0);
        __builtin_amdgcn_sched_barrier(0);
        load(o0, w0, kc + 3);
        __builtin_amdgcn_sched_barrier(0);
        comp(o1, w1);
        __builtin_amdgcn_sched_barrier(0);
        load(o1, w1, kc + 4 < 16 ? kc + 4 : 15);
        __builtin_amdgcn_sched_barrier(0);
        comp(o2, w2);
        __builtin_amdgcn_sched_barrier(0);
    }
    comp(o0, w0);
    f32x4* red = (f32x4*)smem;
    constexpr int NT = NB * 4;
#pragma unroll
    for (int r0 = 0; r0 < NT; r0 += 16) {
        if (r0) __syncthreads();
#pragma unroll
        for (int T = r0; T < (r0 + 16 < NT ? r0 + 16 : NT); ++T) red[((T - r0) * 8 + w) * 64 + lane] = acc[T >> 2][T & 3];
        __syncthreads();
#pragma unroll
        for (int h = 0; h < 2; ++h) {
            const int T = r0 + h * 8 + w;
            if (T < NT) {
                f32x4 s = red[((T - r0) * 8 + 0) * 64 + lane];
#pragma unroll
                for (int k = 1; k < 8; ++k) s += red[((T - r0) * 8 + k) * 64 + lane];
                const int row = (T >> 2) * 16 + (lane & 15), col = (T & 3) * 16 + (lane >> 4) * 4;
                u32x2 o = {cvtpk(s[0], s[1]), cvtpk(s[2], s[3])};
                *(u32x2*)(outp + (size_t)row * 1024 + col) = o;
            }
        }
    }
}

__global__ __launch_bounds__(512, 2) void oproj_k(const char* __restrict__ ws, __hip_bfloat16* __restrict__ out) {
    __shared__ __attribute__((aligned(16))) char smem[16 * 8 * 1024];
    const int bid = blockIdx.x, xcd = bid & 7, idx = bid >> 3;
    const int ib = 2 * xcd + (idx >> 4), half = (idx >> 3) & 1, nt = idx & 7;
    const int inst = ib >> 3, b = ib & 7;
    const int lane = threadIdx.x & 63;
    const int w = __builtin_amdgcn_readfirstlane((int)threadIdx.x >> 6);
    const int rb0 = half ? 7 : 0;
    const char* of = ws + WS_OF + ((size_t)ib * 128 * NRB + rb0) * 1024;
    const char* wf = ws + WS_WF + ((size_t)inst * 128 * 32 + nt * 4) * 1024;
    __hip_bfloat16* outp = out + (size_t)(b * SQ + rb0 * 16) * 1024 + inst * 512 + nt * 64;
    if (half) oproj_body<6>(of, wf, outp, smem, w, lane);
    else oproj_body<7>(of, wf, outp, smem, w, lane);
}

extern "C" void kernel_launch(void* const* d_in, const int* in_sizes, int n_in, void* d_out, int out_size, void* d_ws,
                              size_t ws_size, hipStream_t stream) {
    Ptrs p;
    for (int i = 0; i < 2; ++i) {
        p.x[i] = (const __hip_bfloat16*)d_in[5 * i + 0];
        p.k[i] = (const __hip_bfloat16*)d_in[5 * i + 1];
        p.v[i] = (const __hip_bfloat16*)d_in[5 * i + 2];
        p.wq[i] = (const __hip_bfloat16*)d_in[5 * i + 3];
        p.wo[i] = (const __hip_bfloat16*)d_in[5 * i + 4];
    }
    (void)hipFuncSetAttribute((const void*)attn_k, hipFuncAttributeMaxDynamicSharedMemorySize, LDS1);
    hipLaunchKernelGGL(attn_k, dim3(256), dim3(512), LDS1, stream, p, (char*)d_ws);
    hipLaunchKernelGGL(oproj_k, dim3(256), dim3(512), 0, stream, (const char*)d_ws, (__hip_bfloat16*)d_out);
}
```
